```python
import torch

def _softmax(x, axis=-1):
    return torch.softmax(x, dim=axis)
def _gelu(x):
    return torch.nn.functional.gelu(x, approximate="tanh")
def _silu(x):
    return torch.nn.functional.silu(x)
def _sigmoid(x):
    return torch.sigmoid(x)
def _layernorm(x, axis=-1, eps=1e-5):
    m = x.mean(dim=axis, keepdim=True)
    v = x.var(dim=axis, keepdim=True, unbiased=False)
    return (x - m) / torch.sqrt(v + eps)
def _scatter_add(tgt, idx, src, axis=0):
    return tgt.clone().index_add_(axis, idx, src)
def _segment_sum(src, seg, n):
    out = torch.zeros((n,) + src.shape[1:], dtype=src.dtype)
    return out.index_add_(0, seg, src)
def _pad(x, pw):
    flat = tuple(v for pair in reversed(pw) for v in pair)
    return torch.nn.functional.pad(x, flat)

kernel_name = 'sequential_preLN__ssm_conv_h3__mixer_s_mlp__prompt'

def setup_inputs():

    _w = (lambda a: (lambda t: t.to(torch.bfloat16) if t.is_floating_point() else t)(torch.as_tensor(a)))
    def _A_setup():
        B, S, D = 1, 2048, 576
        ks = 4
        return {
            "q": _w((lambda *s: torch.randn(*s, dtype=torch.bfloat16) * 0.05)(B, S, D)),
            "k": _w((lambda *s: torch.randn(*s, dtype=torch.bfloat16) * 0.4)(B, S, D)),
            "v": _w((lambda *s: torch.randn(*s, dtype=torch.bfloat16) * 0.4)(B, S, D)),
            "shift_k": _w((lambda *s: torch.randn(*s, dtype=torch.bfloat16) * 0.4)(D, ks) * 0.5),
            "ssm_k": _w((lambda *s: torch.randn(*s, dtype=torch.bfloat16) * 0.4)(D, S) * (1.0 / S ** 0.5)),
            "ssm_D": _w((lambda *s: torch.randn(*s, dtype=torch.bfloat16) * 0.4)(D)),
        }
    _A_in = _A_setup()

    def _B_setup():
        B, S, D, F = 1, 2048, 576, 4608
        return {
            "x":  _w((lambda *s: torch.randn(*s, dtype=torch.bfloat16) * 0.05)(B, S, D)),
            "w1": _w((lambda *s: torch.randn(*s, dtype=torch.bfloat16) * 3.2)(D, D) * (D ** -0.5)),
            "w2": _w((lambda *s: torch.randn(*s, dtype=torch.bfloat16) * 3.2)(D, D) * (D ** -0.5)),
            "w3": _w((lambda *s: torch.randn(*s, dtype=torch.bfloat16) * 3.2)(D, F) * (D ** -0.5)),
            "w4": _w((lambda *s: torch.randn(*s, dtype=torch.bfloat16) * 1.6)(F, D) * (F ** -0.5)),
        }
    _B_in = _B_setup()

    return {
        "A_q": _A_in["q"],
        "A_k": _A_in["k"],
        "A_v": _A_in["v"],
        "A_shift_k": _A_in["shift_k"],
        "A_ssm_k": _A_in["ssm_k"],
        "A_ssm_D": _A_in["ssm_D"],
        "B_x": _B_in["x"],
        "B_w1": _B_in["w1"],
        "B_w2": _B_in["w2"],
        "B_w3": _B_in["w3"],
        "B_w4": _B_in["w4"],
    }

def reference(A_q, A_k, A_v, A_shift_k, A_ssm_k, A_ssm_D, B_x, B_w1, B_w2, B_w3, B_w4):
    def _A_ref(q, k, v, shift_k, ssm_k, ssm_D):
        B, S, D = q.shape
        ks = shift_k.shape[-1]
        k_pad = _pad(k, ((0, 0), (ks - 1, 0), (0, 0)))
        k_shift = torch.zeros_like(k)
        for t in range(ks):
            k_shift = k_shift + k_pad[:, t:t + S, :] * shift_k[:, ks - 1 - t][None, None, :]
        kv = k_shift * v
        L = 2 * S
        kv_pad = _pad(kv, ((0, 0), (0, S), (0, 0)))
        kf_pad = _pad(ssm_k, ((0, 0), (0, S)))
        kv_f = (lambda x, axes=(-2,-1): torch.fft.rfft2(x.float(), dim=axes))(kv_pad[:, None, :, :], axes=(1, 2))
        kf_f = (lambda x, axes=(-2,-1): torch.fft.rfft2(x.float(), dim=axes))(kf_pad[:, None, :], axes=(1, 2))
        kf_f = (lambda x, *d: x.permute(*d))(kf_f, 1, 2, 0)
        y_f = kv_f * kf_f[None, :, :, :]
        y = (lambda x, s, axes=(-2,-1): torch.fft.irfft2(x, s=s, dim=axes).to(torch.bfloat16))(y_f, (1, L), axes=(1, 2))[:, 0, :S, :]
        y = y + kv * ssm_D[None, None, :]
        return q * y

    def _B_ref(x, w1, w2, w3, w4):
        B, S, D = x.shape
        h = _gelu(_layernorm(x, axis=-1) @ w1)
        c = D // 4
        g0 = _pad(h[:, :, 0*c:1*c], ((0, 0), (1, 0), (0, 0)))[:, :S, :]
        g1 = _pad(h[:, :, 1*c:2*c], ((0, 0), (0, 1), (0, 0)))[:, 1:, :]
        g2 = _pad(h[:, :, 2*c:3*c], ((0, 0), (2, 0), (0, 0)))[:, :S, :]
        g3 = _pad(h[:, :, 3*c:   ], ((0, 0), (0, 2), (0, 0)))[:, 2:, :]
        hs = torch.concatenate([g0, g1, g2, g3], axis=-1) @ w2
        x1 = x + hs
        h2 = _gelu(_layernorm(x1, axis=-1) @ w3) @ w4
        return x1 + h2

    x = A_q
    h = x + _A_ref(_layernorm(x, axis=-1), A_k, A_v, A_shift_k, A_ssm_k, A_ssm_D)
    h2 = h + _B_ref(_layernorm(h, axis=-1), B_w1, B_w2, B_w3, B_w4)
    return h2
```

```cpp
#include <hip/hip_runtime.h>
#include <hip/hip_bf16.h>
#include <stdint.h>
typedef unsigned short u16;
constexpr int S = 2048, D = 576, F = 4608;
__device__ __forceinline__ float bf2f(u16 b) { return __uint_as_float((unsigned)b << 16); }
__device__ __forceinline__ u16 f2bf(float f) { __hip_bfloat16 h = __float2bfloat16(f); return *(u16*)&h; }
__device__ __forceinline__ float gelu(float x) { float u = 0.7978845608028654f * (x + 0.044715f * x * x * x); return 0.5f * x * (1.f + tanhf(u)); }
__global__ void n_kv(const u16* k, const u16* v, const u16* sk, u16* kv) {
  int i = blockIdx.x * 256 + threadIdx.x; int s = i / D, d = i % D; float a = 0;
  for (int j = 3; j >= 0; j--) if (s - j >= 0) a += bf2f(k[(s - j) * D + d]) * bf2f(sk[d * 4 + j]);
  kv[i] = f2bf(a * bf2f(v[i]));
}
__global__ void n_conv(const u16* kv, const u16* f, const u16* Dv, u16* yv) {
  int i = blockIdx.x * 256 + threadIdx.x; int s = i / D, d = i % D; float a = 0;
  for (int j = 0; j <= s; j++) a += bf2f(kv[(s - j) * D + d]) * bf2f(f[d * S + j]);
  yv[i] = f2bf(bf2f(f2bf(a)) + bf2f(f2bf(bf2f(kv[i]) * bf2f(Dv[d]))));
}
__device__ float blk_sum(float v, float* sh) { for (int o = 32; o; o >>= 1) v += __shfl_xor(v, o); return v; }
__device__ void ln9(float* x, float* o) {
  float s = 0; for (int i = 0; i < 9; i++) s += x[i]; s = blk_sum(s, 0) / D;
  float q = 0; for (int i = 0; i < 9; i++) q += (x[i] - s) * (x[i] - s); q = blk_sum(q, 0) / D;
  float r = rsqrtf(q + 1e-5f); for (int i = 0; i < 9; i++) o[i] = bf2f(f2bf((x[i] - s) * r));
}
__global__ void n_ln1(const u16* x, const u16* yv, u16* h, u16* xb, u16* xn) {
  int s = blockIdx.x, l = threadIdx.x; float a[9], q[9], hh[9], b[9], c[9];
  for (int i = 0; i < 9; i++) a[i] = bf2f(x[s * D + l + 64 * i]);
  ln9(a, q);
  for (int i = 0; i < 9; i++) { hh[i] = bf2f(f2bf(a[i] + bf2f(f2bf(q[i] * bf2f(yv[s * D + l + 64 * i]))))); h[s * D + l + 64 * i] = f2bf(hh[i]); }
  ln9(hh, b); ln9(b, c);
  for (int i = 0; i < 9; i++) { xb[s * D + l + 64 * i] = f2bf(b[i]); xn[s * D + l + 64 * i] = f2bf(c[i]); }
}
__global__ void n_ln(const u16* x, u16* o) {
  int s = blockIdx.x, l = threadIdx.x; float a[9], q[9];
  for (int i = 0; i < 9; i++) a[i] = bf2f(x[s * D + l + 64 * i]);
  ln9(a, q);
  for (int i = 0; i < 9; i++) o[s * D + l + 64 * i] = f2bf(q[i]);
}
template <int MODE> __global__ void n_gemm(const u16* A, const u16* B, u16* C, const u16* R, const u16* R2, int N, int K) {
  __shared__ float As[16][33], Bs[32][17];
  int tx = threadIdx.x & 15, ty = threadIdx.x >> 4; int m = blockIdx.y * 16 + ty, n = blockIdx.x * 16 + tx; float acc = 0;
  for (int k0 = 0; k0 < K; k0 += 32) {
    for (int e = threadIdx.x; e < 512; e += 256) { int r = e >> 5, c = e & 31; int row = blockIdx.y * 16 + r; int kk = k0 + c;
      if (MODE == 1) { int g = kk / 144; int sh = g == 0 ? 1 : g == 1 ? -1 : g == 2 ? 2 : -2; row -= sh; }
      As[r][c] = (row >= 0 && row < S) ? bf2f(A[(size_t)row * K + kk]) : 0.f;
      int br = e >> 4, bc = e & 15; Bs[br][bc] = bf2f(B[(size_t)(k0 + br) * N + blockIdx.x * 16 + bc]); }
    __syncthreads();
    for (int c = 0; c < 32; c++) acc += As[ty][c] * Bs[c][tx];
    __syncthreads();
  }
  float o = bf2f(f2bf(acc));
  if (MODE == 0) o = gelu(o);
  if (MODE == 1) o = bf2f(R[(size_t)m * N + n]) + o;
  if (MODE == 2) o = bf2f(R2[(size_t)m * N + n]) + bf2f(f2bf(bf2f(R[(size_t)m * N + n]) + o));
  C[(size_t)m * N + n] = f2bf(o);
}
#define SZ ((size_t)S * D * 2)
#define DBG_TABLE { {"t_kv", 0*SZ, S, D, 1, 0}, {"t_yv", 1*SZ, S, D, 1, 0}, {"t_h", 2*SZ, S, D, 1, 0}, {"t_xb", 3*SZ, S, D, 1, 0}, {"t_h1", 5*SZ, S, D, 1, 0}, {"t_x1", 6*SZ, S, D, 1, 0} }
extern "C" void kernel_launch(void* const* d_in, const int* in_sizes, int n_in, void* d_out, int out_size, void* d_ws, size_t ws_size, hipStream_t stream) {
  const u16 *q = (const u16*)d_in[0], *k = (const u16*)d_in[1], *v = (const u16*)d_in[2], *sk = (const u16*)d_in[3], *f = (const u16*)d_in[4], *Dv = (const u16*)d_in[5];
  const u16 *w1 = (const u16*)d_in[7], *w2 = (const u16*)d_in[8], *w3 = (const u16*)d_in[9], *w4 = (const u16*)d_in[10];
  u16* ws = (u16*)d_ws; size_t n = (size_t)S * D;
  u16 *kv = ws, *yv = ws + n, *h = ws + 2 * n, *xb = ws + 3 * n, *xn = ws + 4 * n, *h1 = ws + 5 * n, *x1 = ws + 6 * n, *xn2 = ws + 7 * n, *H = ws + 8 * n;
  n_kv<<<n / 256, 256, 0, stream>>>(k, v, sk, kv);
  n_conv<<<n / 256, 256, 0, stream>>>(kv, f, Dv, yv);
  n_ln1<<<S, 64, 0, stream>>>(q, yv, h, xb, xn);
  n_gemm<0><<<dim3(D / 16, S / 16), 256, 0, stream>>>(xn, w1, h1, nullptr, nullptr, D, D);
  n_gemm<1><<<dim3(D / 16, S / 16), 256, 0, stream>>>(h1, w2, x1, xb, nullptr, D, D);
  n_ln<<<S, 64, 0, stream>>>(x1, xn2);
  n_gemm<0><<<dim3(F / 16, S / 16), 256, 0, stream>>>(xn2, w3, H, nullptr, nullptr, F, D);
  n_gemm<2><<<dim3(D / 16, S / 16), 256, 0, stream>>>(H, w4, (u16*)d_out, x1, h, D, F);
}
```

```cpp
#include <hip/hip_runtime.h>
#include <stdint.h>

typedef unsigned short u16;
typedef unsigned v4u __attribute__((ext_vector_type(4)));
typedef unsigned v2u __attribute__((ext_vector_type(2)));
typedef float f32x2 __attribute__((ext_vector_type(2)));
typedef float f32x4 __attribute__((ext_vector_type(4)));
typedef float f32x16 __attribute__((ext_vector_type(16)));
typedef __bf16 bf16x8 __attribute__((ext_vector_type(8)));
typedef __bf16 bf16x2 __attribute__((ext_vector_type(2)));
typedef short s16x4 __attribute__((ext_vector_type(4)));
#define LAS __attribute__((address_space(3)))
#define DEV __device__ __forceinline__

constexpr int S = 2048, D = 576, F = 4608;
constexpr int KS = 18;
constexpr size_t ACT = (size_t)S * D * 2;
constexpr size_t WIN = (size_t)D * D * 2;
constexpr size_t O_UT = 0, O_YV = ACT, O_H = 2 * ACT, O_XB = 3 * ACT, O_HSH = 4 * ACT, O_X1 = 5 * ACT, O_PART = 6 * ACT,
                 O_W1 = 14 * ACT, O_W2 = O_W1 + WIN, O_W3 = O_W2 + WIN, O_W4 = O_W3 + 8 * WIN;
constexpr float EPS = 1e-5f;

DEV unsigned pk(float a, float b) { f32x2 v = {a, b}; return __builtin_bit_cast(unsigned, __builtin_convertvector(v, bf16x2)); }
DEV float lo(unsigned u) { return __builtin_bit_cast(float, u << 16); }
DEV float hi(unsigned u) { return __builtin_bit_cast(float, u & 0xffff0000u); }
DEV float rnd(float a) { return lo(pk(a, a)); }
DEV f32x4 mfma16(v4u a, v4u b, f32x4 c) {
  return __builtin_amdgcn_mfma_f32_16x16x32_bf16(__builtin_bit_cast(bf16x8, a), __builtin_bit_cast(bf16x8, b), c, 0, 0, 0);
}
DEV f32x16 mfma32(v4u a, v4u b, f32x16 c) {
  return __builtin_amdgcn_mfma_f32_32x32x16_bf16(__builtin_bit_cast(bf16x8, a), __builtin_bit_cast(bf16x8, b), c, 0, 0, 0);
}
DEV v2u trrd(LAS char* p) { return __builtin_bit_cast(v2u, __builtin_amdgcn_ds_read_tr16_b64_v4i16((LAS s16x4*)p)); }
DEV float gelu(float x) {
  const float c0 = -2.0f * 0.7978845608028654f * 1.4426950408889634f, c1 = c0 * 0.044715f;
  float t = __builtin_fmaf(x * x, c1, c0);
  float e = __builtin_amdgcn_exp2f(x * t);
  return x * __builtin_amdgcn_rcpf(1.0f + e);
}
DEV void unpack8(v4u u, float* f) {
  f[0] = lo(u.x); f[1] = hi(u.x); f[2] = lo(u.y); f[3] = hi(u.y); f[4] = lo(u.z); f[5] = hi(u.z); f[6] = lo(u.w); f[7] = hi(u.w);
}
DEV v4u pack8(const float* f) { v4u u; u.x = pk(f[0], f[1]); u.y = pk(f[2], f[3]); u.z = pk(f[4], f[5]); u.w = pk(f[6], f[7]); return u; }
DEV float qsum(float v) { v += __shfl_xor(v, 16); v += __shfl_xor(v, 32); return v; }
DEV int pairbase(int g) { return 144 * g + ((g & 1) << 4); }
DEV int singlebase(int g) { return 144 * g + ((g & 1) ? 0 : 128); }

DEV void kv_job(int b, LAS char* lds, const u16* k, const u16* v, const u16* sk, char* ws) {
  const int tid = threadIdx.x, l = tid & 63, w = tid >> 6;
  const int cg = b % 9, rt = b / 9;
  const int piece = tid & 7, rp = tid >> 3, r0 = 128 * rt + 2 * rp, ch0 = 64 * cg + 8 * piece;
  v4u kk[5], vv[2], sw[4];
#pragma unroll
  for (int i = 0; i < 5; i++) { int r = r0 - 3 + i; kk[i] = r >= 0 ? *(const v4u*)(k + (size_t)r * D + ch0) : v4u{0, 0, 0, 0}; }
#pragma unroll
  for (int i = 0; i < 2; i++) vv[i] = *(const v4u*)(v + (size_t)(r0 + i) * D + ch0);
#pragma unroll
  for (int i = 0; i < 4; i++) sw[i] = *(const v4u*)(sk + ch0 * 4 + 8 * i);
  float kf[5][8], vf[2][8], wf[4][8];
#pragma unroll
  for (int i = 0; i < 5; i++) unpack8(kk[i], kf[i]);
#pragma unroll
  for (int i = 0; i < 2; i++) unpack8(vv[i], vf[i]);
#pragma unroll
  for (int i = 0; i < 4; i++) unpack8(sw[i], wf[i]);
#pragma unroll
  for (int rr = 0; rr < 2; rr++) {
    float u[8];
#pragma unroll
    for (int j = 0; j < 8; j++) {
      const float* ww = &wf[j >> 1][4 * (j & 1)];
      float a = kf[rr][j] * ww[3];
      a = __builtin_fmaf(kf[rr + 1][j], ww[2], a);
      a = __builtin_fmaf(kf[rr + 2][j], ww[1], a);
      a = __builtin_fmaf(kf[rr + 3][j], ww[0], a);
      u[j] = a * vf[rr][j];
    }
    *(LAS v4u*)(lds + (2 * rp + rr) * 144 + piece * 16) = pack8(u);
  }
  __syncthreads();
  const int q = l >> 4, a = (l & 15) >> 2, bb = l & 3, cb = w & 3;
#pragma unroll
  for (int it = 0; it < 2; it++) {
    const int tg = (w >> 2) * 8 + 4 * it + q;
    LAS char* p = lds + (8 * tg + 7 - a) * 144 + (16 * cb + 4 * bb) * 2;
    v2u x0 = trrd(p), x1 = trrd(p - 4 * 144);
    v4u o = {x0.x, x0.y, x1.x, x1.y};
    *(v4u*)(ws + O_UT + ((size_t)(64 * cg + 16 * cb + (l & 15)) * S + 128 * rt + 8 * tg) * 2) = o;
  }
}

DEV void retile_job(int jb, LAS char* lds, const u16* w1, const u16* w2, const u16* w3, const u16* w4, char* ws) {
  const int tid = threadIdx.x, l = tid & 63, w = tid >> 6;
  const int wi = jb / 9, kp = jb % 9;
  const u16* src; int ld; char* dst;
  if (wi == 0) { src = w1; ld = D; dst = ws + O_W1; }
  else if (wi == 1) { src = w2; ld = D; dst = ws + O_W2; }
  else if (wi < 10) { src = w3 + D * (wi - 2); ld = F; dst = ws + O_W3 + WIN * (wi - 2); }
  else { src = w4 + (size_t)D * D * (wi - 10); ld = D; dst = ws + O_W4 + WIN * (wi - 10); }
  src += (size_t)64 * kp * ld;
  v4u t[9];
#pragma unroll
  for (int it = 0; it < 9; it++) { int i = tid + 512 * it, row = i / 72, pc = i % 72; t[it] = *(const v4u*)(src + (size_t)row * ld + pc * 8); }
#pragma unroll
  for (int it = 0; it < 9; it++) { int i = tid + 512 * it, row = i / 72, pc = i % 72; *(LAS v4u*)(lds + row * 1168 + pc * 16) = t[it]; }
  __syncthreads();
  const int q = l >> 4, a = (l & 15) >> 2, bb = l & 3;
#pragma unroll
  for (int it = 0; it < 9; it++) {
    const int f = w * 9 + it, ksl = f / 36, T = f % 36, g = T / 9, i = T % 9;
    const int col = i < 8 ? pairbase(g) + 32 * (i >> 1) + 8 * bb + 4 * (i & 1) : singlebase(g) + 4 * bb;
    LAS char* p = lds + (32 * ksl + 8 * q + a) * 1168 + col * 2;
    v2u x0 = trrd(p), x1 = trrd(p + 4 * 1168);
    v4u o = {x0.x, x0.y, x1.x, x1.y};
    const int ks = 2 * kp + ksl;
    int fi;
    if (wi < 2) fi = T * KS + ks;
    else if (i < 4 || i == 8) fi = g * 90 + ks * 5 + (i < 4 ? i : 4);
    else fi = 360 + g * 72 + ks * 4 + (i - 4);
    *(v4u*)(dst + (size_t)fi * 1024 + l * 16) = o;
  }
}

__global__ __launch_bounds__(512) void k0_ingest(const u16* k, const u16* v, const u16* sk, const u16* w1, const u16* w2, const u16* w3,
                                                 const u16* w4, char* ws) {
  __shared__ __attribute__((aligned(16))) char smem[64 * 1168];
  LAS char* lds = (LAS char*)smem;
  const int b = blockIdx.x;
  if (b < 144) kv_job(b, lds, k, v, sk, ws);
  else retile_job(b - 144, lds, w1, w2, w3, w4, ws);
}

constexpr int CU_SLOT = 95 * 80, CF_SLOT = 4192, CV_F = 4 * CU_SLOT, CV_X = CV_F + 4 * CF_SLOT, CV_BYTES = CV_X + 8 * 4096;
__global__ __launch_bounds__(512) void k1_conv(const u16* f, const u16* Dv, char* ws) {
  __shared__ __attribute__((aligned(16))) char smem[CV_BYTES];
  LAS char* lds = (LAS char*)smem;
  const int tid = threadIdx.x, l = tid & 63, w = __builtin_amdgcn_readfirstlane(tid >> 6);
  const int wg = blockIdx.x, oct = wg / 3, part = wg % 3, chb = 8 * oct + 2 * part;
  v4u tu[2], tf[2];
#pragma unroll
  for (int it = 0; it < 2; it++) {
    const int i = tid + 512 * it;
    tu[it] = *(const v4u*)(ws + O_UT + (size_t)chb * S * 2 + i * 16);
    tf[it] = *(const v4u*)((const char*)f + (size_t)chb * S * 2 + i * 16);
  }
  for (int i = tid; i < 4 * 155; i += 512) *(LAS v4u*)(lds + (i / 155) * CU_SLOT + (i % 155) * 16) = v4u{0, 0, 0, 0};
  if (tid < 24) { const int sl = tid / 6, pc = tid % 6; *(LAS v4u*)(lds + CV_F + sl * CF_SLOT + (pc < 5 ? pc * 16 : 80 + 4096)) = v4u{0, 0, 0, 0}; }
#pragma unroll
  for (int it = 0; it < 2; it++) {
    const int i = tid + 512 * it, sl = i >> 8, pc = i & 255;
    *(LAS v4u*)(lds + sl * CU_SLOT + (31 + (pc >> 2)) * 80 + (pc & 3) * 16) = tu[it];
    *(LAS v4u*)(lds + CV_F + sl * CF_SLOT + 80 + pc * 16) = tf[it];
  }
  const int u24 = 8 * part + w, c8 = u24 / 3, r3 = u24 % 3;
  const int type = c8 == 2 ? (r3 + 1) % 3 : r3;
  const int slot = c8 - 2 * part, nt = type != 0, cb = type == 2 ? 32 : 0;
  const int lam = l & 31, h = l >> 5;
  const float dcoef = lo((unsigned)Dv[chb + slot]);
  __syncthreads();
  v4u R[17];
  {
    const int e0 = 32 * (cb - 1 + 16 * h) + lam + 33;
    LAS unsigned* fp = (LAS unsigned*)(lds + CV_F + slot * CF_SLOT) + (e0 >> 1);
    const unsigned sh = (e0 & 1) * 16;
#pragma unroll
    for (int m = 0; m < 17; m++) {
      unsigned d0 = fp[16 * m], d1 = fp[16 * m + 1], d2 = fp[16 * m + 2], d3 = fp[16 * m + 3], d4 = fp[16 * m + 4];
      R[m].x = __builtin_amdgcn_alignbit(d1, d0, sh); R[m].y = __builtin_amdgcn_alignbit(d2, d1, sh);
      R[m].z = __builtin_amdgcn_alignbit(d3, d2, sh); R[m].w = __builtin_amdgcn_alignbit(d4, d3, sh);
    }
  }
  f32x16 acc[4];
#pragma unroll
  for (int s = 0; s < 4; s++)
#pragma unroll
    for (int i = 0; i < 16; i++) acc[s][i] = 0.f;
  LAS char* ub = lds + slot * CU_SLOT + (31 + 32 * nt + lam - cb - 16 * h - 15) * 80;
#pragma unroll
  for (int m = 0; m < 16; m++) {
#pragma unroll
    for (int s = 0; s < 4; s++) {
      v4u A = R[m + 1];
      if (s) { const bool c = lam < 32 - 8 * s; A.x = c ? A.x : R[m].x; A.y = c ? A.y : R[m].y; A.z = c ? A.z : R[m].z; A.w = c ? A.w : R[m].w; }
      v4u B = *(LAS v4u*)(ub + (15 - m) * 80 + 16 * s);
      acc[s] = mfma32(A, B, acc[s]);
    }
  }
  float y[16];
#pragma unroll
  for (int i = 0; i < 16; i++) y[i] = (acc[0][i] + acc[1][(i + 12) & 15]) + (acc[2][(i + 8) & 15] + acc[3][(i + 4) & 15]);
  if (type == 2) {
#pragma unroll
    for (int o = 0; o < 4; o++) *(LAS f32x4*)(lds + CV_X + w * 4096 + o * 1024 + l * 16) = f32x4{y[4 * o], y[4 * o + 1], y[4 * o + 2], y[4 * o + 3]};
  }
  __syncthreads();
  if (type != 2) {
    if (type == 1) {
#pragma unroll
      for (int o = 0; o < 4; o++) { f32x4 t = *(LAS f32x4*)(lds + CV_X + (w + 1) * 4096 + o * 1024 + l * 16); y[4 * o] += t.x; y[4 * o + 1] += t.y; y[4 * o + 2] += t.z; y[4 * o + 3] += t.w; }
    }
    const int blk = 32 * nt + lam;
    char* dst = ws + O_YV + ((size_t)(chb + slot) * S + 32 * blk + 4 * h) * 2;
#pragma unroll
    for (int o = 0; o < 4; o++) {
      v2u uu = *(LAS v2u*)(lds + slot * CU_SLOT + (31 + blk) * 80 + 16 * o + (h ? 0 : 8));
      float r0 = rnd(y[4 * o]) + rnd(hi(uu.y) * dcoef), r1 = rnd(y[4 * o + 1]) + rnd(lo(uu.y) * dcoef);
      float r2 = rnd(y[4 * o + 2]) + rnd(hi(uu.x) * dcoef), r3v = rnd(y[4 * o + 3]) + rnd(lo(uu.x) * dcoef);
      *(v2u*)(dst + 16 * o) = v2u{pk(r0, r1), pk(r2, r3v)};
    }
  }
}

template <bool GELU>
DEV void small_gemm(const v4u (&wa)[KS], const v4u (&wb)[KS], LAS char* bimg, LAS char* oimg, int w, int l, int g) {
  f32x4 a0 = {0, 0, 0, 0}, a1 = {0, 0, 0, 0}, a2 = {0, 0, 0, 0};
  if (w < 2) {
#pragma unroll
    for (int ks = 0; ks < KS; ks++) {
      v4u b0 = *(LAS v4u*)(bimg + ks * 1024 + l * 16), b1 = *(LAS v4u*)(bimg + (KS + ks) * 1024 + l * 16);
      a0 = mfma16(wa[ks], b0, a0); a1 = mfma16(wa[ks], b1, a1); a2 = mfma16(wb[ks], w ? b1 : b0, a2);
    }
  } else {
#pragma unroll
    for (int ks = 0; ks < KS; ks++) {
      v4u b0 = *(LAS v4u*)(bimg + ks * 1024 + l * 16), b1 = *(LAS v4u*)(bimg + (KS + ks) * 1024 + l * 16);
      a0 = mfma16(wa[ks], b0, a0); a1 = mfma16(wa[ks], b1, a1);
    }
  }
  const int tok = l & 15, q = l >> 4, odd = g & 1;
  auto fin = [&](f32x4 a) { if (GELU) return v2u{pk(gelu(a.x), gelu(a.y)), pk(gelu(a.z), gelu(a.w))}; else return v2u{pk(a.x, a.y), pk(a.z, a.w)}; };
  const int pc = 2 * odd + 4 * (w >> 1) + q;
  *(LAS v2u*)(oimg + (pc * 32 + tok) * 16 + (w & 1) * 8) = fin(a0);
  *(LAS v2u*)(oimg + (pc * 32 + 16 + tok) * 16 + (w & 1) * 8) = fin(a1);
  if (w < 2) { const int ps = (odd ? 0 : 16) + (q >> 1); *(LAS v2u*)(oimg + (ps * 32 + 16 * w + tok) * 16 + (q & 1) * 8) = fin(a2); }
}
DEV size_t frag_off(int row, int colpiece) { return ((size_t)((row >> 4) * KS + (colpiece >> 2)) * 64 + (colpiece & 3) * 16 + (row & 15)) * 16; }

constexpr int G1_Y = 0, G1_B = 576 * 64, G1_O = G1_B + 36 * 1024, G1_ST = G1_O + 18 * 512, G1_BYTES = G1_ST + 3 * 8 * 16 * 8;
__global__ __launch_bounds__(512) void k2_g1(const u16* x, char* ws) {
  __shared__ __attribute__((aligned(16))) char smem[G1_BYTES];
  LAS char* lds = (LAS char*)smem;
  const int tid = threadIdx.x, l = tid & 63, w = __builtin_amdgcn_readfirstlane(tid >> 6);
  const int b = blockIdx.x, tile = (b & 7) * 8 + (b >> 5), g = (b >> 3) & 3, t0 = 32 * tile;
  const int tok = l & 15, q = l >> 4, rt = w & 1, kq = w >> 1, nk = kq < 2 ? 5 : 4;
  v4u ty[5], tx[5];
#pragma unroll
  for (int it = 0; it < 5; it++) { const int i = tid + 512 * it; if (i < 2304) ty[it] = *(const v4u*)(ws + O_YV + ((size_t)(i >> 2) * S + t0) * 2 + (i & 3) * 16); }
#pragma unroll
  for (int it = 0; it < 5; it++) { const int ks = kq + 4 * it; tx[it] = it < nk ? *(const v4u*)(x + (size_t)(t0 + 16 * rt + tok) * D + 32 * ks + 8 * q) : v4u{0, 0, 0, 0}; }
  v4u wa[KS], wb[KS];
  {
    const char* wp = ws + O_W1 + (size_t)(9 * g + w) * KS * 1024 + l * 16;
    const char* wq = ws + O_W1 + (size_t)(9 * g + 8) * KS * 1024 + l * 16;
#pragma unroll
    for (int ks = 0; ks < KS; ks++) wa[ks] = *(const v4u*)(wp + ks * 1024);
    if (w < 2) {
#pragma unroll
      for (int ks = 0; ks < KS; ks++) wb[ks] = *(const v4u*)(wq + ks * 1024);
    }
  }
#pragma unroll
  for (int it = 0; it < 5; it++) { const int i = tid + 512 * it; if (i < 2304) *(LAS v4u*)(lds + G1_Y + i * 16) = ty[it]; }
  float xv[5][8];
#pragma unroll
  for (int it = 0; it < 5; it++) unpack8(tx[it], xv[it]);
  auto stats = [&](int stage, float& mean, float& rstd) {
    float s = 0, ss = 0;
#pragma unroll
    for (int it = 0; it < 5; it++)
#pragma unroll
      for (int j = 0; j < 8; j++) { s += xv[it][j]; ss = __builtin_fmaf(xv[it][j], xv[it][j], ss); }
    s = qsum(s); ss = qsum(ss);
    LAS f32x2* st = (LAS f32x2*)(lds + G1_ST) + stage * 128;
    if (q == 0) st[(rt * 16 + tok) * 4 + kq] = f32x2{s, ss};
    __syncthreads();
    f32x4 p0 = *(LAS f32x4*)(st + (rt * 16 + tok) * 4), p1 = *(LAS f32x4*)(st + (rt * 16 + tok) * 4 + 2);
    s = (p0.x + p0.z) + (p1.x + p1.z); ss = (p0.y + p0.w) + (p1.y + p1.w);
    mean = s * (1.0f / D);
    const float var = fmaxf(ss * (1.0f / D) - mean * mean, 0.f);
    rstd = __builtin_amdgcn_rsqf(var + EPS);
  };
  float mean, rstd;
  stats(0, mean, rstd);
  const int a = (l & 15) >> 2, bb = l & 3;
  const int row = t0 + 16 * rt + tok;
#pragma unroll
  for (int it = 0; it < 5; it++) {
    const int ks = it < nk ? kq + 4 * it : 0;
    LAS char* p = lds + G1_Y + (32 * ks + 8 * q + a) * 64 + (16 * rt + 4 * bb) * 2;
    v2u y0 = trrd(p), y1 = trrd(p + 4 * 64);
    float yv[8]; unpack8(v4u{y0.x, y0.y, y1.x, y1.y}, yv);
#pragma unroll
    for (int j = 0; j < 8; j++) xv[it][j] = it < nk ? xv[it][j] + rnd(rnd((xv[it][j] - mean) * rstd) * yv[j]) : 0.f;
    v4u hp = pack8(xv[it]);
    unpack8(hp, xv[it]);
    const int cp = 4 * ks + q;
    if (it < nk && cp / 18 == g) *(v4u*)(ws + O_H + frag_off(row, cp)) = hp;
  }
  stats(1, mean, rstd);
#pragma unroll
  for (int it = 0; it < 5; it++) {
    const int ks = kq + 4 * it;
#pragma unroll
    for (int j = 0; j < 8; j++) xv[it][j] = it < nk ? (xv[it][j] - mean) * rstd : 0.f;
    v4u hp = pack8(xv[it]);
    unpack8(hp, xv[it]);
    const int cp = 4 * ks + q;
    if (it < nk && cp / 18 == g) *(v4u*)(ws + O_XB + frag_off(row, cp)) = hp;
  }
  stats(2, mean, rstd);
#pragma unroll
  for (int it = 0; it < 5; it++) {
    const int ks = kq + 4 * it;
    float t[8];
#pragma unroll
    for (int j = 0; j < 8; j++) t[j] = (xv[it][j] - mean) * rstd;
    if (it < nk) *(LAS v4u*)(lds + G1_B + (rt * KS + ks) * 1024 + l * 16) = pack8(t);
  }
  __syncthreads();
  small_gemm<true>(wa, wb, lds + G1_B, lds + G1_O, w, l, g);
  __syncthreads();
  const int dl = g == 0 ? 1 : g == 1 ? -1 : g == 2 ? 2 : -2;
#pragma unroll
  for (int it = 0; it < 2; it++) {
    const int i = tid + 512 * it;
    if (i < 576) {
      const int pc = i >> 5, t = t0 + (i & 31);
      v4u o = *(LAS v4u*)(lds + G1_O + i * 16);
      int tt = t + dl;
      if (tt < 0) { tt += S; o = v4u{0, 0, 0, 0}; }
      else if (tt >= S) { tt -= S; o = v4u{0, 0, 0, 0}; }
      *(v4u*)(ws + O_HSH + frag_off(tt, 18 * g + pc)) = o;
    }
  }
}

constexpr int G2_B = 0, G2_O = 36 * 1024, G2_BYTES = G2_O + 18 * 512;
__global__ __launch_bounds__(512) void k3_g2(char* ws) {
  __shared__ __attribute__((aligned(16))) char smem[G2_BYTES];
  LAS char* lds = (LAS char*)smem;
  const int tid = threadIdx.x, l = tid & 63, w = __builtin_amdgcn_readfirstlane(tid >> 6);
  const int b = blockIdx.x, tile = (b & 7) * 8 + (b >> 5), g = (b >> 3) & 3, t0 = 32 * tile;
  v4u tb[5], xb[2];
#pragma unroll
  for (int it = 0; it < 5; it++) { const int i = tid + 512 * it; if (i < 2304) tb[it] = *(const v4u*)(ws + O_HSH + (size_t)tile * 36 * 1024 + i * 16); }
  v4u wa[KS], wb[KS];
  {
    const char* wp = ws + O_W2 + (size_t)(9 * g + w) * KS * 1024 + l * 16;
    const char* wq = ws + O_W2 + (size_t)(9 * g + 8) * KS * 1024 + l * 16;
#pragma unroll
    for (int ks = 0; ks < KS; ks++) wa[ks] = *(const v4u*)(wp + ks * 1024);
    if (w < 2) {
#pragma unroll
      for (int ks = 0; ks < KS; ks++) wb[ks] = *(const v4u*)(wq + ks * 1024);
    }
  }
#pragma unroll
  for (int it = 0; it < 2; it++) { const int i = tid + 512 * it; if (i < 576) xb[it] = *(const v4u*)(ws + O_XB + frag_off(t0 + (i & 31), 18 * g + (i >> 5))); }
#pragma unroll
  for (int it = 0; it < 5; it++) { const int i = tid + 512 * it; if (i < 2304) *(LAS v4u*)(lds + G2_B + i * 16) = tb[it]; }
  __syncthreads();
  small_gemm<false>(wa, wb, lds + G2_B, lds + G2_O, w, l, g);
  __syncthreads();
#pragma unroll
  for (int it = 0; it < 2; it++) {
    const int i = tid + 512 * it;
    if (i < 576) {
      float a[8], c[8];
      unpack8(*(LAS v4u*)(lds + G2_O + i * 16), a); unpack8(xb[it], c);
#pragma unroll
      for (int j = 0; j < 8; j++) a[j] += c[j];
      *(v4u*)(ws + O_X1 + frag_off(t0 + (i & 31), 18 * g + (i >> 5))) = pack8(a);
    }
  }
}

constexpr int ML_X = 0, ML_H = 72 * 1024, ML_ST = 144 * 1024, ML_BYTES = ML_ST + 64 * 2 * 8;
template <int NT, int WK>
DEV void mlp_body(LAS char* lds, char* ws, int j, int tile, int w, int l) {
  const int g = w & 3, tok = l & 15, q = l >> 4, odd = g & 1;
  const size_t run = (NT == 5 ? (size_t)g * 90 : 360 + (size_t)g * 72) * 1024 + l * 16;
  const char* w3 = ws + O_W3 + WIN * j + run;
  const char* w4 = ws + O_W4 + WIN * j + run;
  const int rot = tile % KS;
  auto kmap = [&](int s) { int k = s + rot; return k >= KS ? k - KS : k; };
  auto wptr = [&](int s) { return s < KS ? w3 + (size_t)kmap(s) * NT * 1024 : w4 + (size_t)kmap(s - KS) * NT * 1024; };
  v4u ring[WK][NT];
#pragma unroll
  for (int s = 0; s < WK; s++) { const char* p = wptr(s);
#pragma unroll
    for (int i = 0; i < NT; i++) ring[s][i] = *(const v4u*)(p + i * 1024); }
  {
    const int rt = w & 3, kh = w >> 2;
    v4u tx[9];
#pragma unroll
    for (int it = 0; it < 9; it++) tx[it] = *(const v4u*)(ws + O_X1 + ((size_t)(4 * tile + rt) * KS + 9 * kh + it) * 1024 + l * 16);
    float s = 0, ss = 0;
#pragma unroll
    for (int it = 0; it < 9; it++) { float t[8]; unpack8(tx[it], t);
#pragma unroll
      for (int jj = 0; jj < 8; jj++) { s += t[jj]; ss = __builtin_fmaf(t[jj], t[jj], ss); } }
    s = qsum(s); ss = qsum(ss);
    LAS f32x2* st = (LAS f32x2*)(lds + ML_ST);
    if (q == 0) st[(rt * 16 + tok) * 2 + kh] = f32x2{s, ss};
    __syncthreads();
    f32x4 p = *(LAS f32x4*)(st + (rt * 16 + tok) * 2);
    const float mean = (p.x + p.z) * (1.0f / D), var = fmaxf((p.y + p.w) * (1.0f / D) - mean * mean, 0.f), rstd = __builtin_amdgcn_rsqf(var + EPS);
#pragma unroll
    for (int it = 0; it < 9; it++) { float t[8]; unpack8(tx[it], t);
#pragma unroll
      for (int jj = 0; jj < 8; jj++) t[jj] = (t[jj] - mean) * rstd;
      *(LAS v4u*)(lds + ML_X + ((9 * kh + it) * 4 + rt) * 1024 + l * 16) = pack8(t); }
    __syncthreads();
  }
  f32x4 acc[NT][4];
#pragma unroll
  for (int ph = 0; ph < 2; ph++) {
#pragma unroll
    for (int i = 0; i < NT; i++)
#pragma unroll
      for (int m = 0; m < 4; m++) acc[i][m] = f32x4{0, 0, 0, 0};
    LAS char* img = lds + (ph ? ML_H : ML_X) + l * 16;
#pragma unroll
    for (int s = 0; s < KS; s++) {
      const int gs = ph * KS + s;
      LAS char* bp = img + kmap(s) * 4096;
      v4u bf[4];
#pragma unroll
      for (int m = 0; m < 4; m++) bf[m] = *(LAS v4u*)(bp + m * 1024);
      v4u cur[NT];
#pragma unroll
      for (int i = 0; i < NT; i++) cur[i] = ring[gs % WK][i];
      if (gs + WK < 2 * KS) { const char* p = wptr(gs + WK);
#pragma unroll
        for (int i = 0; i < NT; i++) ring[gs % WK][i] = *(const v4u*)(p + i * 1024); }
#pragma unroll
      for (int i = 0; i < NT; i++)
#pragma unroll
        for (int m = 0; m < 4; m++) acc[i][m] = mfma16(cur[i], bf[m], acc[i][m]);
      __builtin_amdgcn_sched_barrier(0);
    }
    const int ks0 = pairbase(g) / 32 + (NT == 5 ? 0 : 2), kss = singlebase(g) / 32, ls = (2 * odd + (q >> 1)) * 16 + tok;
    if (ph == 0) {
#pragma unroll
      for (int pp = 0; pp < 2; pp++)
#pragma unroll
        for (int m = 0; m < 4; m++) {
          f32x4 a = acc[2 * pp][m], c = acc[2 * pp + 1][m];
          v4u o = {pk(gelu(a.x), gelu(a.y)), pk(gelu(a.z), gelu(a.w)), pk(gelu(c.x), gelu(c.y)), pk(gelu(c.z), gelu(c.w))};
          *(LAS v4u*)(lds + ML_H + ((ks0 + pp) * 4 + m) * 1024 + l * 16) = o;
        }
      if (NT == 5) {
#pragma unroll
        for (int m = 0; m < 4; m++) { f32x4 a = acc[NT - 1][m];
          *(LAS v2u*)(lds + ML_H + (kss * 4 + m) * 1024 + ls * 16 + (q & 1) * 8) = v2u{pk(gelu(a.x), gelu(a.y)), pk(gelu(a.z), gelu(a.w))}; }
      }
      __syncthreads();
    } else {
      char* dst = ws + O_PART + ACT * j + (size_t)4 * tile * KS * 1024;
#pragma unroll
      for (int pp = 0; pp < 2; pp++)
#pragma unroll
        for (int m = 0; m < 4; m++) {
          f32x4 a = acc[2 * pp][m], c = acc[2 * pp + 1][m];
          *(v4u*)(dst + (m * KS + ks0 + pp) * 1024 + l * 16) = v4u{pk(a.x, a.y), pk(a.z, a.w), pk(c.x, c.y), pk(c.z, c.w)};
        }
      if (NT == 5) {
#pragma unroll
        for (int m = 0; m < 4; m++) { f32x4 a = acc[NT - 1][m];
          *(v2u*)(dst + (m * KS + kss) * 1024 + ls * 16 + (q & 1) * 8) = v2u{pk(a.x, a.y), pk(a.z, a.w)}; }
      }
    }
  }
}
__global__ __launch_bounds__(512, 2) void k4_mlp(char* ws) {
  __shared__ __attribute__((aligned(16))) char smem[ML_BYTES];
  LAS char* lds = (LAS char*)smem;
  const int tid = threadIdx.x, l = tid & 63, w = __builtin_amdgcn_readfirstlane(tid >> 6);
  const int b = blockIdx.x, j = b & 7, tile = b >> 3;
  if (w < 4) mlp_body<5, 5>(lds, ws, j, tile, w, l);
  else mlp_body<4, 6>(lds, ws, j, tile, w, l);
}

__global__ __launch_bounds__(256) void k5_reduce(const char* ws, u16* out) {
  const int i = blockIdx.x * 256 + threadIdx.x;
  v4u p[8];
#pragma unroll
  for (int j = 0; j < 8; j++) p[j] = *(const v4u*)(ws + O_PART + ACT * j + (size_t)i * 16);
  v4u x1 = *(const v4u*)(ws + O_X1 + (size_t)i * 16), hh = *(const v4u*)(ws + O_H + (size_t)i * 16);
  float s[8], t[8];
  unpack8(p[0], s);
#pragma unroll
  for (int j = 1; j < 8; j++) { unpack8(p[j], t);
#pragma unroll
    for (int e = 0; e < 8; e++) s[e] += t[e]; }
  unpack8(x1, t);
#pragma unroll
  for (int e = 0; e < 8; e++) s[e] = rnd(t[e] + rnd(s[e]));
  unpack8(hh, t);
#pragma unroll
  for (int e = 0; e < 8; e++) s[e] += t[e];
  const int ln = i & 63, fr = i >> 6, rt = fr / KS, ks = fr % KS;
  *(v4u*)(out + (size_t)(16 * rt + (ln & 15)) * D + 32 * ks + 8 * (ln >> 4)) = pack8(s);
}

#define DBG_TABLE { {"t_kv", O_UT, S, D, 1, 3}, {"t_yv", O_YV, S, D, 1, 1}, {"t_h", O_H, S, D, 1, 2}, {"t_xb", O_XB, S, D, 1, 2}, {"t_x1", O_X1, S, D, 1, 2} }
extern "C" void kernel_launch(void* const* d_in, const int* in_sizes, int n_in, void* d_out, int out_size, void* d_ws, size_t ws_size,
                              hipStream_t stream) {
  const u16 *q = (const u16*)d_in[0], *k = (const u16*)d_in[1], *v = (const u16*)d_in[2], *sk = (const u16*)d_in[3], *f = (const u16*)d_in[4],
            *Dv = (const u16*)d_in[5], *w1 = (const u16*)d_in[7], *w2 = (const u16*)d_in[8], *w3 = (const u16*)d_in[9], *w4 = (const u16*)d_in[10];
  char* ws = (char*)d_ws;
  k0_ingest<<<144 + 162, 512, 0, stream>>>(k, v, sk, w1, w2, w3, w4, ws);
  k1_conv<<<216, 512, 0, stream>>>(f, Dv, ws);
  k2_g1<<<256, 512, 0, stream>>>(q, ws);
  k3_g2<<<256, 512, 0, stream>>>(ws);
  k4_mlp<<<256, 512, 0, stream>>>(ws);
  k5_reduce<<<S * D / 8 / 256, 256, 0, stream>>>(ws, (u16*)d_out);
}
```

```cpp
#include <hip/hip_runtime.h>
#include <stdint.h>
typedef unsigned short u16;
typedef unsigned v4u __attribute__((ext_vector_type(4)));
typedef unsigned v2u __attribute__((ext_vector_type(2)));
typedef float f32x2 __attribute__((ext_vector_type(2)));
typedef float f32x4 __attribute__((ext_vector_type(4)));
typedef float f32x16 __attribute__((ext_vector_type(16)));
typedef __bf16 bf16x8 __attribute__((ext_vector_type(8)));
typedef __bf16 bf16x2 __attribute__((ext_vector_type(2)));
typedef short s16x4 __attribute__((ext_vector_type(4)));
typedef __amdgpu_buffer_rsrc_t rsrc_t;
#define LAS __attribute__((address_space(3)))
#define DEV __device__ __forceinline__
#define SB() __builtin_amdgcn_sched_barrier(0)
#define INL __attribute__((always_inline))
template <int V> struct IC { static constexpr int v = V; };
template <int I, int N, class Fn> DEV void sfor(Fn&& f) { if constexpr (I < N) { f(IC<I>{}); sfor<I + 1, N>(f); } }

constexpr int S = 2048, D = 576, F = 4608;
constexpr int KS = 18;
constexpr int ACT = S * D * 2;
constexpr int WIN = D * D * 2;
constexpr int O_EW = 0, O_FLAG = 1024, O_UT = 16384, O_YV = O_UT + ACT, O_W1 = O_YV + ACT, O_W2 = O_W1 + WIN, O_HSH = O_YV + 3840 * 1024,
              O_X1 = O_HSH + ACT, O_PART = O_X1 + ACT, O_WM = O_PART + 8 * ACT, WS_USED = O_WM + 16 * WIN;
static_assert(O_W2 + WIN <= O_HSH, "ws map");
constexpr float EPS = 1e-5f;
constexpr unsigned SENT = 0xffffffffu, EPOCH_STEP = 0x9E3779B9u, NSTAGE = 3;
constexpr int SPIN_CAP = 1 << 14;
constexpr int NCONV = 216;
#define NPRE1 10
#define NPRE2 10

constexpr int L_X = 0, L_H = 73728, L_HS = 147456, L_ST = 157760, L_CS = L_ST + 2048, L_MISC = L_CS + 1024, L_BYTES = L_MISC + 64;
constexpr int L_XH = 0, L_RT = 36864;
constexpr int CU_SLOT = 95 * 80, CF_SLOT = 4192, CX_SLOT = 32 * 144;
constexpr int L_CU = L_H, L_CF = L_CU + 4 * CU_SLOT, L_CX = L_CF + 4 * CF_SLOT;
constexpr int L_Y = L_H, L_B2 = L_H, L_A9B = L_H + 36896;
static_assert(L_CX + 8 * CX_SLOT <= L_ST && L_BYTES <= 163840 && L_A9B <= L_CX && L_A9B + 18432 <= L_HS, "lds map");

DEV unsigned pk(float a, float b) { f32x2 v = {a, b}; return __builtin_bit_cast(unsigned, __builtin_convertvector(v, bf16x2)); }
DEV float lo(unsigned u) { return __builtin_bit_cast(float, u << 16); }
DEV float hi(unsigned u) { return __builtin_bit_cast(float, u & 0xffff0000u); }
DEV float asf(unsigned u) { return __builtin_bit_cast(float, u); }
DEV unsigned asu(float f) { return __builtin_bit_cast(unsigned, f); }
DEV float dot2(unsigned a, unsigned b, float c) {
  return __builtin_amdgcn_fdot2_f32_bf16(__builtin_bit_cast(bf16x2, a), __builtin_bit_cast(bf16x2, b), c, false);
}
DEV f32x4 mfma16(v4u a, v4u b, f32x4 c) {
  return __builtin_amdgcn_mfma_f32_16x16x32_bf16(__builtin_bit_cast(bf16x8, a), __builtin_bit_cast(bf16x8, b), c, 0, 0, 0);
}
DEV f32x16 mfma32(v4u a, v4u b, f32x16 c) {
  return __builtin_amdgcn_mfma_f32_32x32x16_bf16(__builtin_bit_cast(bf16x8, a), __builtin_bit_cast(bf16x8, b), c, 0, 0, 0);
}
DEV v2u trrd(LAS char* p) { return __builtin_bit_cast(v2u, __builtin_amdgcn_ds_read_tr16_b64_v4i16((LAS s16x4*)p)); }
DEV v4u ldws(rsrc_t rs, int voff, int soff) { return __builtin_amdgcn_raw_buffer_load_b128(rs, voff, soff, 16); }
DEV void stws(rsrc_t rs, v4u v, int voff, int soff) { __builtin_amdgcn_raw_buffer_store_b128(v, rs, voff, soff, 16); }
DEV void bar() { asm volatile("s_waitcnt lgkmcnt(0)\n\ts_barrier" ::: "memory"); }
DEV void drain() { asm volatile("s_waitcnt vmcnt(0)" ::: "memory"); }
DEV void swap16(unsigned& a, unsigned& b) { auto r = __builtin_amdgcn_permlane16_swap(a, b, false, false); unsigned x = r[0], y = r[1]; a = x; b = y; }
DEV void swap32(unsigned& a, unsigned& b) { auto r = __builtin_amdgcn_permlane32_swap(a, b, false, false); unsigned x = r[0], y = r[1]; a = x; b = y; }
DEV void glds16(const void* g, int ldsaddr) {
  unsigned keep;
  asm volatile("s_mov_b32 %0, m0\n\ts_mov_b32 m0, %2\n\ts_nop 0\n\tglobal_load_lds_dwordx4 %1, off\n\ts_mov_b32 m0, %0"
               : "=&s"(keep) : "v"(g), "s"(ldsaddr) : "memory");
}
DEV int lane() { int l; asm volatile("v_mbcnt_lo_u32_b32 %0, -1, 0\n\tv_mbcnt_hi_u32_b32 %0, -1, %0" : "=v"(l)); return l; }
template <class T> DEV void pin(T& v) { asm volatile("" : "+v"(v)); }
DEV unsigned mx4(unsigned m, v4u v) { m = max(max(m, v.x), v.y); return max(max(m, v.z), v.w); }
template <int N> DEV void vmwait() { asm volatile("s_waitcnt vmcnt(%0)" :: "n"(N) : "memory"); }
DEV float gelu_ln(float y, f32x4 c) {
  const float p = __builtin_fmaf(y * y, c.y, c.x);
  const float e = __builtin_amdgcn_exp2f(y * p);
  return y * __builtin_amdgcn_rcpf(__builtin_fmaf(e, c.z, c.z));
}
DEV f32x4 gelu_consts(float r, float rinv) {
  const float c0 = -2.0f * 0.7978845608028654f * 1.4426950408889634f, c1 = c0 * 0.044715f;
  return f32x4{r * c0, r * r * r * c1, rinv, 0.f};
}
DEV float stat_reduce(float s, float ss) {
  unsigned a = asu(s), b = asu(ss);
  swap16(a, b);
  float t = asf(a) + asf(b);
  unsigned c = asu(t), d = c;
  swap32(c, d);
  return asf(c) + asf(d);
}
DEV void stat_acc(v4u v, float& s, float& ss) {
  const unsigned one = 0x3f803f80u;
  s = dot2(v.x, one, s); ss = dot2(v.x, v.x, ss); s = dot2(v.y, one, s); ss = dot2(v.y, v.y, ss);
  s = dot2(v.z, one, s); ss = dot2(v.z, v.z, ss); s = dot2(v.w, one, s); ss = dot2(v.w, v.w, ss);
}
DEV int pairbase(int g) { return 144 * g + ((g & 1) << 4); }
DEV int singlebase(int g) { return 144 * g + ((g & 1) ? 0 : 128); }
DEV int frag_off(int row, int colpiece) { return (((row >> 4) * KS + (colpiece >> 2)) * 64 + (colpiece & 3) * 16 + (row & 15)) * 16; }

DEV v4u load_flags(rsrc_t rs, int b, int l) { return ldws(rs, l * 16, O_FLAG + (b & 7) * 1024); }
DEV void wait_flags(rsrc_t rs, v4u f, int b, int l, unsigned epoch, unsigned stage, uint64_t need, int& alive) {
  const unsigned base = epoch + stage, lim = NSTAGE - stage;
  auto bad = [&]() INL { return __builtin_amdgcn_ballot_w64(max(max(f.x - base, f.y - base), max(f.z - base, f.w - base)) > lim) & need; };
  uint64_t any = bad();
#pragma nounroll
  while (any != 0 && alive > 0) {
    asm volatile("s_sleep 1" ::: "memory");
    f = load_flags(rs, b, l);
    --alive;
    any = bad();
  }
  asm volatile("" ::: "memory");
}
DEV void publish_flag(rsrc_t rs, int b, int l, unsigned val) {
  if (l < 8) __builtin_amdgcn_raw_buffer_store_b32(val, rs, l * 1024 + b * 4, O_FLAG, 16);
}
template <int N, class OFF, class USE>
DEV void settle(rsrc_t rs, v4u (&d)[N], OFF off, USE use, int& alive) {
  unsigned m = 0;
#pragma unroll
  for (int i = 0; i < N; i++) { m = mx4(m, d[i]); use(i, d[i]); }
  uint64_t any = __builtin_amdgcn_ballot_w64(m == SENT);
#pragma nounroll
  while (any != 0 && alive > 0) {
    asm volatile("" ::: "memory");
    if (m == SENT) {
#pragma unroll
      for (int i = 0; i < N; i++) d[i] = off(i);
      m = 0;
#pragma unroll
      for (int i = 0; i < N; i++) { m = mx4(m, d[i]); use(i, d[i]); }
    }
    --alive;
    any = __builtin_amdgcn_ballot_w64(m == SENT);
  }
}

struct Job { int kind, j, s; };
DEV Job job_main(int n) { Job jb; jb.kind = 2 + (n >= 144); n -= (n >= 144) * 144; jb.j = n & 7; jb.s = n >> 3; return jb; }
DEV int slot_ks(int s) {
  if (s == 12) return 4;
  if (s == 13) return 13;
  const int g = (s < 14 ? s : s - 14) & 3, k0 = pairbase(g) >> 5;
  return k0 + (s < 4 ? 0 : s < 8 ? 2 : s < 12 ? 1 : 3);
}
struct WPtr { const u16 *w1, *w2, *w3, *w4; };
DEV void retile_load(Job jb, int stage, int r0, int rstep, int n, int l, const WPtr& wp) {
  const u16* src; int ld = D;
  if (jb.kind < 2) src = (jb.kind ? wp.w2 : wp.w1) + 32 * jb.s;
  else if (jb.kind == 2) { src = wp.w3 + D * jb.j + 32 * slot_ks(jb.s); ld = F; }
  else src = wp.w4 + D * jb.j * D + 32 * slot_ks(jb.s);
  src += (l >> 2) * ld + (l & 3) * 8;
#pragma unroll
  for (int it = 0; it < 9; it++) {
    const int r = r0 + rstep * it;
    if (it < n && r < KS) {
      const u16* p = src + 32 * (jb.kind == 3 ? slot_ks(r) : r) * ld;
      glds16(p, stage + r * 2048); glds16(p + 16 * ld, stage + r * 2048 + 1024);
    }
  }
}
DEV void retile_read(Job jb, LAS char* lds, int stage, int w, int l, v4u (&fr)[3][2]) {
  const int q = l >> 4, a = (l & 15) >> 2, bb = l & 3;
  const bool nat = jb.kind < 2 || jb.s == 12 || jb.s == 13;
  LAS char* p = lds + stage + w * 2048 + (8 * q + a) * 64 + (nat ? 8 * bb : 16 * bb);
  const int ts = nat ? 32 : 8;
#pragma unroll
  for (int it = 0; it < 3; it++)
#pragma unroll
    for (int t = 0; t < 2; t++) {
      if (it < 2 || w < 2) { v2u x0 = trrd(p + it * 16384 + t * ts), x1 = trrd(p + it * 16384 + t * ts + 256); fr[it][t] = v4u{x0.x, x0.y, x1.x, x1.y}; }
      else fr[it][t] = v4u{0, 0, 0, 0};
    }
  if (!(jb.kind & 1)) {
    const v4u ones = {0x3f803f80u, 0x3f803f80u, 0x3f803f80u, 0x3f803f80u};
#pragma unroll
    for (int t = 0; t < 2; t++) {
      f32x4 c = {0, 0, 0, 0};
#pragma unroll
      for (int it = 0; it < 3; it++) c = mfma16(ones, fr[it][t], c);
      *(LAS float*)(lds + L_CS + (t * 16 + (l & 15)) * 32 + w * 4) = c.x;
    }
  }
}
DEV void retile_emit(Job jb, LAS char* lds, rsrc_t rs, int w, int l, v4u (&fr)[3][2]) {
  if (!(jb.kind & 1)) {
#pragma unroll
    for (int t = 0; t < 2; t++) {
      const f32x4 p0 = *(LAS f32x4*)(lds + L_CS + (t * 16 + (l & 15)) * 32), p1 = *(LAS f32x4*)(lds + L_CS + (t * 16 + (l & 15)) * 32 + 16);
      const float mu = (((p0.x + p0.y) + (p0.z + p0.w)) + ((p1.x + p1.y) + (p1.z + p1.w))) * (1.0f / D);
#pragma unroll
      for (int it = 0; it < 3; it++) if (it < 2 || w < 2) {
        const unsigned c0 = fr[it][t].x, c1 = fr[it][t].y, c2 = fr[it][t].z, c3 = fr[it][t].w;
        fr[it][t] = v4u{pk(lo(c0) - mu, hi(c0) - mu), pk(lo(c1) - mu, hi(c1) - mu), pk(lo(c2) - mu, hi(c2) - mu), pk(lo(c3) - mu, hi(c3) - mu)};
      }
    }
  }
  const int s = jb.s;
#pragma unroll
  for (int t = 0; t < 2; t++) {
    const int ww = s < 8 ? s : s < 12 ? s - 8 : s < 14 ? 2 * (s - 12) + t : s - 10, ti = s < 8 ? t : (s == 12 || s == 13) ? 4 : 2 + t;
    const int nt = ww < 4 ? 5 : 4, bs = jb.j * 1296 + (ww < 4 ? ww * 180 : 144 + ww * 144);
#pragma unroll
    for (int it = 0; it < 3; it++) if (it < 2 || w < 2) {
      const int r = w + 8 * it;
      int o;
      if (jb.kind < 2) o = (jb.kind ? O_W2 : O_W1) + ((2 * s + t) * KS + r) * 1024;
      else {
        const int c0 = jb.kind == 2 ? 0 : 32 * nt, c1 = jb.kind == 2 ? 36 : 32 * nt + 8, rr = jb.kind == 2 ? r : r - 14;
        const int f = rr < 0 ? 18 * nt + r * nt + ti : ti < 2 ? c0 + rr * 2 + ti : c1 + rr * (nt - 2) + ti - 2;
        o = O_WM + (bs + f) * 1024;
      }
      stws(rs, fr[it][t], l * 16, o);
    }
  }
}

template <bool NINE, int A9S, class SIDE>
DEV void small_gemm_t(const v4u (&wa)[21], LAS char* p0, LAS char* p1, LAS char* a9, f32x4& a0, f32x4& a1, f32x4& a2, SIDE side) {
  v4u b0[3], b1[3], wn[3];
  auto rd = [&](int ks) INL {
    b0[ks % 3] = *(LAS v4u*)(p0 + ks * 1024); b1[ks % 3] = *(LAS v4u*)(p1 + ks * 1024);
    if (NINE) wn[ks % 3] = *(LAS v4u*)(a9 + ks * A9S);
  };
  rd(0); rd(1);
  SB();
  sfor<0, KS>([&](auto k_) INL {
    constexpr int ks = decltype(k_)::v;
    if constexpr (ks + 2 < KS) rd(ks + 2);
    a0 = mfma16(wa[ks], b0[ks % 3], a0); a1 = mfma16(wa[ks], b1[ks % 3], a1);
    if constexpr (NINE) a2 = mfma16(wn[ks % 3], b0[ks % 3], a2);
    side(k_);
    SB();
  });
}
template <int A9S, class SIDE>
DEV void small_gemm(const v4u (&wa)[21], LAS char* bimg, LAS char* a9, int w, int l, f32x4& a0, f32x4& a1, f32x4& a2, SIDE side) {
  LAS char* p = bimg + l * 16;
  if (w < 2) {
    f32x4 x = w ? a1 : a0, y = w ? a0 : a1;
    small_gemm_t<true, A9S>(wa, p + w * KS * 1024, p + (w ^ 1) * KS * 1024, a9 + l * 16, x, y, a2, side);
    a0 = w ? y : x; a1 = w ? x : y;
  } else small_gemm_t<false, A9S>(wa, p, p + KS * 1024, a9, a0, a1, a2, side);
}
DEV int ks9(int w, int i) { return i < 2 || w < 2 ? w + 8 * i : w + 8; }
DEV v4u load_gw(rsrc_t rs, int base, int g, int w, int l, int i) {
  return i < KS ? ldws(rs, l * 16 + (i & 3) * 1024, base + ((9 * g + w) * KS + (i & ~3)) * 1024)
                : ldws(rs, l * 16, base + ((9 * g + 8) * KS + ks9(w, i - KS)) * 1024);
}
DEV v4u piece2(f32x4 a, f32x4 b) {
  unsigned a0 = pk(a.x, a.y), a1 = pk(a.z, a.w), b0 = pk(b.x, b.y), b1 = pk(b.z, b.w);
  swap16(a0, b0); swap16(a1, b1);
  return v4u{a0, a1, b0, b1};
}

template <int NT> struct Seg {
  static constexpr int steps(int s) { return s < 2 ? 18 : s == 2 ? 8 : s == 3 ? 6 : 4; }
  static constexpr int nt(int s) { return s == 0 || s == 4 ? 2 : s == 1 || s == 5 ? NT - 2 : NT; }
  static constexpr int base(int s) { int b = 0; for (int i = 0; i < s; i++) b += steps(i) * nt(i); return b; }
  static constexpr int slot0(int s) { return s < 3 ? 0 : s == 3 ? 8 : 14; }
};
constexpr int RW = 16;
DEV v4u ring_load(rsrc_t rs, int sbase, int l16, int f) { return ldws(rs, l16 + (f & 3) * 1024, sbase + (f & ~3) * 1024); }

template <int NT>
DEV void mlp_body(LAS char* lds, rsrc_t rs, v4u (&ring)[RW], const v4u (&tx)[9], int sbase, int b, int w, unsigned epoch, v4u& fl
) {
  using G = Seg<NT>;
  constexpr int TOT = 36 * NT;
  const int g = w & 3, odd = g & 1, j = b & 7, tile = b >> 3;
  const int l16 = lane() * 16;
  LAS char* const bx = lds + L_X + l16;
  LAS char* const bh = lds + L_H + l16;
  auto bptr = [&](int s, int st) INL { return (s < 2 ? bx : bh) + (G::slot0(s) + st) * 4096; };
  f32x4 acc1[NT][4], acc2[NT][4];
#pragma unroll
  for (int i = 0; i < NT; i++)
#pragma unroll
    for (int m = 0; m < 4; m++) { acc1[i][m] = f32x4{0, 0, 0, 0}; acc2[i][m] = f32x4{0, 0, 0, 0}; }
  v4u bf[4];
#pragma unroll
  for (int m = 0; m < 4; m++) bf[m] = *(LAS v4u*)(bx + m * 1024);
  float t0 = 0, t1 = 0, sa = 0, sb = 0, qa = 0, qb = 0;
  f32x4 gc[2];
  unsigned go[4];
  LAS char *gcp = lds, *hp = lds, *hs = lds;
  const v4u sent = {SENT, SENT, SENT, SENT};
  auto gelu_chunk = [&](auto c_, auto tb_, auto per_) INL {
    constexpr int c = decltype(c_)::v, tb = decltype(tb_)::v, PER = decltype(per_)::v, P = c / 10, k = c % 10, m = P / PER, e = 2 * (P % PER);
    const f32x4 av = acc1[e < 8 ? tb + (e >> 2) : NT - 1][m], kc = gc[m & 1];
    const float y0 = av[e & 3], y1 = av[(e & 3) + 1];
    if constexpr (k == 0) { t0 = y0 * y0; pin(t0); t1 = y1 * y1; }
    if constexpr (k == 1) { t0 = __builtin_fmaf(t0, kc.y, kc.x); pin(t0); t1 = __builtin_fmaf(t1, kc.y, kc.x); }
    if constexpr (k == 2 || k == 8) { t0 *= y0; pin(t0); t1 *= y1; }
    if constexpr (k == 3) t0 = __builtin_amdgcn_exp2f(t0);
    if constexpr (k == 4) t1 = __builtin_amdgcn_exp2f(t1);
    if constexpr (k == 5) { t0 = __builtin_fmaf(t0, kc.z, kc.z); pin(t0); t1 = __builtin_fmaf(t1, kc.z, kc.z); }
    if constexpr (k == 6) t0 = __builtin_amdgcn_rcpf(t0);
    if constexpr (k == 7) t1 = __builtin_amdgcn_rcpf(t1);
    if constexpr (k == 9) {
      go[(e >> 1) & 3] = pk(t0, t1);
      if constexpr (e == 6) *(LAS v4u*)(hp + m * 1024) = v4u{go[0], go[1], go[2], go[3]};
      if constexpr (e == 10) *(LAS v2u*)(hs + m * 1024) = v2u{go[0], go[1]};
      if constexpr (e == 0 && m < 3) gc[(m + 1) & 1] = *(LAS f32x4*)(gcp + (m + 1) * 256);
    }
  };
  auto gelu_setup = [&](int slot, bool single) INL {
    gcp = lds + L_ST + (l16 & 255);
    gc[0] = *(LAS f32x4*)gcp;
    hp = bh + slot * 4096;
    if (single) hs = lds + L_H + (12 + (g >> 1)) * 4096 + (2 * odd + (l16 >> 9)) * 256 + (l16 & 255) + ((l16 >> 5) & 8);
  };
  auto stat_chunk = [&](auto c_) INL {
    constexpr int c = decltype(c_)::v, d = c / 3, k = c % 3;
    if constexpr (k == 0) { const unsigned v = tx[d >> 2][d & 3]; t0 = lo(v); t1 = hi(v); }
    if constexpr (k == 1) { sa += t0; pin(sa); sb += t1; }
    if constexpr (k == 2) { qa = __builtin_fmaf(t0, t0, qa); pin(qa); qb = __builtin_fmaf(t1, t1, qb); }
  };
  const int pdst = O_PART + j * ACT + 4 * tile * KS * 1024, ks0 = (pairbase(g) >> 5) + (NT == 5 ? 0 : 2);
  auto store_pair = [&](int pp, int m) INL {
    f32x4 a = acc2[2 * pp][m], c = acc2[2 * pp + 1][m];
    stws(rs, v4u{pk(a.x, a.y), pk(a.z, a.w), pk(c.x, c.y), pk(c.z, c.w)}, l16, pdst + (m * KS + ks0 + pp) * 1024);
  };
  sfor<0, 6>([&](auto s_) INL {
    constexpr int s = decltype(s_)::v, nst = G::steps(s), n = G::nt(s), tb = (s == 1 || s == 5) ? 2 : 0;
    constexpr int NCH = s == 0 ? 108 : s == 1 ? 160 : s == 2 && NT == 5 ? 240 : s == 3 && NT == 4 ? 160 : 0;
    constexpr int SPAN = s == 0 ? 116 : (nst - 1) * 4 * n - 2;
    sfor<0, nst>([&](auto st_c) INL {
      constexpr int st = decltype(st_c)::v;
      constexpr bool last = st == nst - 1;
      if constexpr (last && s == 1) vmwait<RW>();
      if constexpr (last && s < 4) bar();
      if constexpr (last && s == 1 && NT == 4) { if (w == 7) publish_flag(rs, b, l16 >> 4, epoch + 3); }
      if constexpr (last && s == 0) gelu_setup(w, false);
      if constexpr (last && s == (NT == 5 ? 1 : 2)) gelu_setup(NT == 5 ? 8 + g : 14 + g, NT == 5);
      constexpr int f0 = G::base(s) + st * n;
      LAS char* np = last ? (s < 5 ? bptr(s + 1, 0) : lds) : bptr(s, st + 1);
      sfor<0, 4 * n>([&](auto k_) INL {
        constexpr int k = decltype(k_)::v, m = k / n, i = k % n, u = 4 * n * st + k, f = f0 + i;
        if constexpr (s < 2) acc1[tb + i][m] = mfma16(ring[f % RW], bf[m], acc1[tb + i][m]);
        else acc2[tb + i][m] = mfma16(ring[f % RW], bf[m], acc2[tb + i][m]);
        if constexpr (i == n - 1 && !(last && s == 5)) bf[m] = *(LAS v4u*)(np + m * 1024);
        if constexpr (m == 3 && f + RW < TOT) ring[f % RW] = ring_load(rs, sbase, l16, f + RW);
        if constexpr (NCH > 0 && u < SPAN) sfor<u * NCH / SPAN, (u + 1) * NCH / SPAN>([&](auto c_) INL {
          if constexpr (s == 0) stat_chunk(c_);
          else gelu_chunk(c_, IC<(s == 1 ? 0 : 2)>{}, IC<(s == 2 ? 6 : 4)>{});
        });
        if constexpr (s == 0 && u == 118) {
          sa += sb; qa += qb;
          sa += asf(__builtin_amdgcn_update_dpp(0, asu(sa), 0x128, 0xf, 0xf, false));
          qa += asf(__builtin_amdgcn_update_dpp(0, asu(qa), 0x128, 0xf, 0xf, false));
        }
        if constexpr (s == 0 && u == 120) sa = stat_reduce(sa, qa);
        if constexpr (s == 0 && u == 123) {
          unsigned a = asu(sa), c = a;
          swap16(a, c);
          const float mean = asf(a) * (1.0f / D);
          qa = fmaxf(asf(c) * (1.0f / D) - mean * mean, 0.f) + EPS;
        }
        if constexpr (s == 0 && u == 126) { const float r = __builtin_amdgcn_rsqf(qa); *(LAS f32x4*)(lds + L_ST + w * 128 + (l16 & 127)) = gelu_consts(r, qa * r); }
        if constexpr (s < 2 && k == 1 && (18 * s + st) % 3 == 1 && 18 * s + st < 27) stws(rs, sent, l16, pdst + (w * 9 + (18 * s + st) / 3) * 1024);
        if constexpr (s == 4 && u == 0) fl = load_flags(rs, b, l16 >> 4);
        if constexpr (s == 5 && st == 0 && i == 0) store_pair(0, m);
        if constexpr (s == 5 && last && m > 0 && i == n - 1) store_pair(1, m - 1);
        SB();
      });
    });
  });
  store_pair(1, 3);
  if (NT == 5) {
    const int l = lane(), q = l >> 4, cp = (singlebase(g) >> 3) + (q >> 1);
#pragma unroll
    for (int m = 0; m < 4; m += 2) stws(rs, piece2(acc2[4][m], acc2[4][m + 1]), frag_off(16 * (m + (q & 1)) + (l & 15), cp), pdst);
  }
}

__global__ __launch_bounds__(512, 2) void mega(const u16* __restrict__ x, const u16* __restrict__ kin, const u16* __restrict__ vin,
                                               const u16* __restrict__ sk, const u16* __restrict__ filt, const u16* __restrict__ Dv,
                                               const u16* __restrict__ w1, const u16* __restrict__ w2, const u16* __restrict__ w3,
                                               const u16* __restrict__ w4, char* ws, u16* out) {
  __shared__ __attribute__((aligned(16))) char smem[L_BYTES];
  LAS char* lds = (LAS char*)smem;
  const int ldsb = (int)(uintptr_t)lds;
  const int tid = threadIdx.x, l = tid & 63, w = __builtin_amdgcn_readfirstlane(tid >> 6), b = blockIdx.x;
  const rsrc_t rs = __builtin_amdgcn_make_buffer_rsrc((void*)ws, 0, WS_USED, 0x00020000);
  const v4u sent = {SENT, SENT, SENT, SENT};
  const WPtr wp = {w1, w2, w3, w4};
  const bool conv = b < NCONV;
  const int tok = l & 15, q = l >> 4;
  int alive = SPIN_CAP;

  unsigned epoch = 0;
  const int pb = 8 * (b / 3) + 2 * (b % 3);
  auto chan = [&](int p) INL { return p < 288 ? 2 * p : 2 * (p - 288) + 1; };
  Job je; je.kind = (b - NCONV) & 1; je.j = 0; je.s = (b - NCONV) >> 1;
  const bool early = !conv && b < NCONV + 36;
  {
    unsigned kk[11], vv[8], dv = 0; v4u sw;
    const int cp = min(tid, 287);
    if (w < 5) {
      const unsigned* kp = (const unsigned*)(kin + (8 * b - 3) * D) + cp;
      const unsigned* vp = (const unsigned*)(vin + 8 * b * D) + cp;
#pragma unroll
      for (int i = 0; i < 11; i++) kk[i] = (i >= 3 || b > 0) ? kp[i * (D / 2)] : 0u;
#pragma unroll
      for (int i = 0; i < 8; i++) vv[i] = vp[i * (D / 2)];
      sw = *(const v4u*)(sk + cp * 8);
      if (conv && w == 4) dv = Dv[chan(pb + (l & 3))];
    } else {
      if (w == 7) epoch = __builtin_amdgcn_raw_buffer_load_b32(rs, 0, O_EW, 16) + EPOCH_STEP;
#pragma unroll
      for (int i = 0; i < 8; i++) { const int n = (w - 5) * 8 + i; stws(rs, sent, l * 16, n < 9 ? O_UT + b * 9216 + n * 1024 : O_YV + b * 15360 + (n - 9) * 1024); }
      if (w < 7 && conv) {
#pragma unroll
        for (int i = 0; i < 8; i++) { const int sl = 2 * (w - 5) + (i >> 2); glds16(filt + chan(pb + sl) * S + ((i & 3) * 64 + l) * 8, ldsb + L_CF + sl * CF_SLOT + 80 + (i & 3) * 1024); }
        vmwait<8>();
      } else if (w < 7 && early) { retile_load(je, ldsb + L_RT, 9 * (w - 5), 1, 9, l, wp); vmwait<18>(); }
      else drain();
    }
    if (conv) {
      for (int i = tid; i < 4 * 155; i += 512) *(LAS v4u*)(lds + L_CU + (i / 155) * CU_SLOT + (i % 155) * 16) = v4u{0, 0, 0, 0};
      if (tid < 24) { const int sl = tid / 6, pc = tid % 6; *(LAS v4u*)(lds + L_CF + sl * CF_SLOT + (pc < 5 ? pc * 16 : 80 + 4096)) = v4u{0, 0, 0, 0}; }
    }
    bar();
    if (w == 7) { publish_flag(rs, b, l, epoch + 1); *(LAS unsigned*)(lds + L_MISC + 16) = epoch; }
    if (w < 5) {
      float u0[8], u1[8];
      unsigned ql[11], qh[11];
#pragma unroll
      for (int i = 1; i < 11; i++) { ql[i] = __builtin_amdgcn_perm(kk[i - 1], kk[i], 0x05040100u); qh[i] = __builtin_amdgcn_perm(kk[i - 1], kk[i], 0x07060302u); }
#pragma unroll
      for (int r = 0; r < 8; r++) {
        u0[r] = dot2(ql[r + 3], sw.x, dot2(ql[r + 1], sw.y, 0.f)) * lo(vv[r]);
        u1[r] = dot2(qh[r + 3], sw.z, dot2(qh[r + 1], sw.w, 0.f)) * hi(vv[r]);
      }
      if (tid < 288) {
        stws(rs, v4u{pk(u0[7], u0[6]), pk(u0[5], u0[4]), pk(u0[3], u0[2]), pk(u0[1], u0[0])}, cp * 16, O_UT + b * 9216);
        stws(rs, v4u{pk(u1[7], u1[6]), pk(u1[5], u1[4]), pk(u1[3], u1[2]), pk(u1[1], u1[0])}, cp * 16, O_UT + b * 9216 + 4608);
      }
      if (w == 4) *(LAS unsigned*)(lds + L_MISC + (l & 3) * 4) = dv << 16;
    }
    if (w == 5 || w == 6) { if (early) drain(); }
    if (w == 7) {
      wait_flags(rs, load_flags(rs, b, l), b, l, epoch, 1, ~0ull, alive);
      if (b == 0 && l == 0) __builtin_amdgcn_raw_buffer_store_b32(epoch, rs, 0, O_EW, 16);
    }
    bar();
  }

  const int tile64 = b >> 3, g = b & 3, t32 = b >> 2, t0 = 32 * t32;
  const int rt = w & 1, kq = w >> 1;
  v4u wa[21];
  const Job jm = job_main(b);
  auto stage_late = [&]() INL {
    retile_load(jm, ldsb + L_RT, w, 8, 3, l, wp);
#pragma unroll
    for (int it = 0; it < 5; it++) if (it < 4 || kq < 2)
      glds16(x + (t0 + 16 * rt + (l >> 2)) * D + 32 * (kq + 4 * it) + 8 * (l & 3), ldsb + L_XH + (rt * KS + kq + 4 * it) * 1024);
  };
  if (conv) {
    const int part = b % 3, u24 = 8 * part + w, c8 = u24 / 3, r3 = u24 % 3;
    const int type = c8 == 2 ? (r3 + 1) % 3 : r3;
    const int slot = c8 - 2 * part, nt = type != 0, cb = type == 2 ? 32 : 0;
    const int lam = l & 31, h = l >> 5;
    v4u tu[2];
    auto off = [&](int i) INL { return ldws(rs, ((tid >> 2) * D + (tid & 3)) * 16, O_UT + (i * 128 * D + pb) * 16); };
    tu[0] = off(0); tu[1] = off(1);
    settle<2>(rs, tu, off, [&](int i, v4u v) INL { *(LAS v4u*)(lds + L_CU + (tid & 3) * CU_SLOT + (31 + 32 * i + (tid >> 4)) * 80 + ((tid >> 2) & 3) * 16) = v; }, alive);
    bar();
    const int e0 = 32 * (cb - 1 + 16 * h) + lam + 33;
    LAS unsigned* fp = (LAS unsigned*)(lds + L_CF + slot * CF_SLOT) + (e0 >> 1);
    const unsigned sh = (e0 & 1) * 16;
    LAS char* ub = lds + L_CU + slot * CU_SLOT + (31 + 32 * nt + lam - cb - 16 * h - 15) * 80;
    unsigned dd[2][5];
    v4u R[2], bq[2][4];
    auto rdf = [&](int m) INL {
#pragma unroll
      for (int i = 0; i < 5; i++) dd[m & 1][i] = fp[16 * m + i];
    };
    auto mkr = [&](int m) INL {
      const unsigned* d = dd[m & 1];
      R[m & 1] = v4u{__builtin_amdgcn_alignbit(d[1], d[0], sh), __builtin_amdgcn_alignbit(d[2], d[1], sh), __builtin_amdgcn_alignbit(d[3], d[2], sh), __builtin_amdgcn_alignbit(d[4], d[3], sh)};
    };
    auto rdb = [&](int m) INL {
#pragma unroll
      for (int s = 0; s < 4; s++) bq[m & 1][s] = *(LAS v4u*)(ub + (15 - m) * 80 + 16 * s);
    };
    rdf(0); rdf(1); rdb(0);
    v2u uu[4];
#pragma unroll
    for (int o = 0; o < 4; o++) uu[o] = *(LAS v2u*)(lds + L_CU + slot * CU_SLOT + (31 + 32 * nt + lam) * 80 + 16 * o + (h ? 0 : 8));
    const float dcoef = type == 2 ? 0.f : *(LAS float*)(lds + L_MISC + slot * 4);
    if (b < 192) {
#pragma unroll
      for (int i = 0; i < 3; i++) stws(rs, sent, l * 16 + i * 1024, O_HSH + (b * 8 + w) * 3072);
    }
    f32x16 acc[4];
#pragma unroll
    for (int s = 1; s < 4; s++)
#pragma unroll
      for (int i = 0; i < 16; i++) acc[s][i] = 0.f;
#pragma unroll
    for (int o = 0; o < 4; o++) { acc[0][4 * o] = hi(uu[o].y) * dcoef; acc[0][4 * o + 1] = lo(uu[o].y) * dcoef; acc[0][4 * o + 2] = hi(uu[o].x) * dcoef; acc[0][4 * o + 3] = lo(uu[o].x) * dcoef; }
    mkr(0);
    SB();
    sfor<0, 16>([&](auto m_) INL {
      constexpr int m = decltype(m_)::v;
      mkr(m + 1);
      if constexpr (m < 15) { rdf(m + 2); rdb(m + 1); }
      acc[0] = mfma32(R[(m + 1) & 1], bq[m & 1][0], acc[0]);
#pragma unroll
      for (int s = 1; s < 4; s++) {
        const bool c = lam < 32 - 8 * s;
        const v4u A = R[(m + 1) & 1], Z = R[m & 1];
        acc[s] = mfma32(v4u{c ? A.x : Z.x, c ? A.y : Z.y, c ? A.z : Z.z, c ? A.w : Z.w}, bq[m & 1][s], acc[s]);
      }
      SB();
    });
#pragma unroll
    for (int o = 0; o < 4; o++) {
      f32x4 y;
#pragma unroll
      for (int e = 0; e < 4; e++) { const int i = 4 * o + e; y[e] = (acc[0][i] + acc[1][(i + 12) & 15]) + (acc[2][(i + 8) & 15] + acc[3][(i + 4) & 15]); }
      *(LAS f32x4*)(lds + L_CX + w * CX_SLOT + lam * 144 + 32 * o + 16 * h) = y;
    }
    vmwait<0>();
    bar();
    LAS char* cx = lds + L_CX + (l >> 2) * 144 + (l & 3) * 32;
    const int yo = (l >> 2) * D * 64 + (l & 3) * 16, ys = O_YV + (32 * nt * D + pb + slot) * 64;
    if (type == 0) {
#pragma unroll
      for (int i = 0; i < 2; i++) {
        const f32x4 a = *(LAS f32x4*)(cx + w * CX_SLOT + i * 2304), c = *(LAS f32x4*)(cx + w * CX_SLOT + i * 2304 + 16);
        stws(rs, v4u{pk(a.x, a.y), pk(a.z, a.w), pk(c.x, c.y), pk(c.z, c.w)}, yo, ys + i * 16 * D * 64);
      }
    } else {
      LAS char* p = cx + (w + 1 - type) * CX_SLOT + (type - 1) * 2304;
      const f32x4 a = *(LAS f32x4*)p + *(LAS f32x4*)(p + CX_SLOT), c = *(LAS f32x4*)(p + 16) + *(LAS f32x4*)(p + CX_SLOT + 16);
      stws(rs, v4u{pk(a.x, a.y), pk(a.z, a.w), pk(c.x, c.y), pk(c.z, c.w)}, yo, ys + (type - 1) * 16 * D * 64);
    }
    stage_late();
#pragma unroll
    for (int i = 0; i < 21; i++) wa[i] = load_gw(rs, O_W1, g, w, l, i);
  } else {
    v4u fr[3][2];
    const Job jx = job_main(b + 40);
    if (b < NCONV + 32) retile_load(jx, ldsb + L_H, w, 8, 3, l, wp);
    if (early) {
      retile_read(je, lds, L_RT, w, l, fr);
      bar();
      retile_emit(je, lds, rs, w, l, fr);
    }
    stage_late();
    if (b < NCONV + 32) {
      vmwait<8>();
      retile_read(jx, lds, L_H, w, l, fr);
      retile_emit(jx, lds, rs, w, l, fr);
    }
#pragma unroll
    for (int i = 0; i < 21; i++) wa[i] = load_gw(rs, O_W1, g, w, l, i);
    bar();
  }

  f32x4 a0 = {0, 0, 0, 0}, a1 = {0, 0, 0, 0}, a2 = {0, 0, 0, 0};
  v4u fl = sent;
  {
    const int pcp = 18 * g + 2 * w + (q >> 1), pcp9 = 18 * g + 16 + (q >> 1), prow = 16 * (q & 1) + tok;
    v4u ty[5], tx[5];
    auto off = [&](int i) INL { return ldws(rs, min(tid + 512 * i, 2303) * 16, O_YV + t32 * 36864); };
#pragma unroll
    for (int i = 0; i < 5; i++) ty[i] = off(i);
    vmwait<26>();
    v4u fr[3][2];
    {
      float sx = 0, sxx = 0;
#pragma unroll
      for (int it = 0; it < 5; it++) { tx[it] = *(LAS v4u*)(lds + L_XH + (rt * KS + min(kq + 4 * it, kq + 12 + (kq < 2 ? 4 : 0))) * 1024 + (tok * 4 + q) * 16); if (it < 4 || kq < 2) stat_acc(tx[it], sx, sxx); }
      const float t = stat_reduce(sx, sxx);
      if (l < 32) *(LAS float*)(lds + L_ST + (rt * 16 + tok) * 32 + kq * 8 + q * 4) = t;
    }
    retile_read(jm, lds, L_RT, w, l, fr);
    bar();
    retile_emit(jm, lds, rs, w, l, fr);
    float xf[5][8]; unsigned qm[5][8];
    {
      f32x4 p0 = *(LAS f32x4*)(lds + L_ST + (rt * 16 + tok) * 32), p1 = *(LAS f32x4*)(lds + L_ST + (rt * 16 + tok) * 32 + 16);
      const float mean = ((p0.x + p0.z) + (p1.x + p1.z)) * (1.0f / D);
      const float r = __builtin_amdgcn_rsqf(fmaxf(((p0.y + p0.w) + (p1.y + p1.w)) * (1.0f / D) - mean * mean, 0.f) + EPS), mr = -mean * r;
#pragma unroll
      for (int it = 0; it < 5; it++) {
        const unsigned c[4] = {tx[it].x, tx[it].y, tx[it].z, tx[it].w};
#pragma unroll
        for (int e = 0; e < 4; e++) {
          xf[it][2 * e] = lo(c[e]); xf[it][2 * e + 1] = hi(c[e]);
          const unsigned qq = pk(__builtin_fmaf(xf[it][2 * e], r, mr), __builtin_fmaf(xf[it][2 * e + 1], r, mr));
          qm[it][2 * e] = qq & 0xffffu; qm[it][2 * e + 1] = qq & 0xffff0000u;
          pin(xf[it][2 * e]); pin(xf[it][2 * e + 1]); pin(qm[it][2 * e]); pin(qm[it][2 * e + 1]);
        }
      }
    }
    settle<21>(rs, wa, [&](int i) INL { return load_gw(rs, O_W1, g, w, l, i); },
               [&](int i, v4u v) INL { if (i >= KS) *(LAS v4u*)(lds + L_RT + ks9(w, i - KS) * 2048 + l * 16) = v; }, alive);
    settle<5>(rs, ty, off, [&](int i, v4u v) INL { const int n = min(tid + 512 * i, 2303); *(LAS v4u*)(lds + L_Y + n * 16 + (n >= 1152 ? 32 : 0)) = v; }, alive);
    bar();
    const int a = (l & 15) >> 2, bb = l & 3;
    float sh = 0, shh = 0;
#pragma unroll
    for (int it = 0; it < 5; it++) if (it < 4 || kq < 2) {
      const int ks = kq + 4 * it;
      LAS char* p = lds + L_Y + (16 * ks + 4 * q + (a >> 1)) * 64 + (a & 1) * (288 * 64 + 32) + rt * 32 + bb * 8;
      v2u y0 = trrd(p), y1 = trrd(p + 128);
      const unsigned yy[4] = {y0.x, y0.y, y1.x, y1.y};
      unsigned hp[4];
#pragma unroll
      for (int e = 0; e < 4; e++) hp[e] = pk(dot2(qm[it][2 * e], yy[e], xf[it][2 * e]), dot2(qm[it][2 * e + 1], yy[e], xf[it][2 * e + 1]));
      const v4u hv = {hp[0], hp[1], hp[2], hp[3]};
      *(LAS v4u*)(lds + L_XH + (rt * KS + ks) * 1024 + l * 16) = hv;
      stat_acc(hv, sh, shh);
    }
    { const float t = stat_reduce(sh, shh); if (l < 32) *(LAS float*)(lds + L_ST + 1024 + (rt * 16 + tok) * 32 + kq * 8 + q * 4) = t; }
    drain();
    bar();
    if (w == 7) publish_flag(rs, b, l, epoch + 2);
    f32x4 gc[2]; float r1[2], mr1[2];
#pragma unroll
    for (int m = 0; m < 2; m++) {
      f32x4 p0 = *(LAS f32x4*)(lds + L_ST + 1024 + (m * 16 + tok) * 32), p1 = *(LAS f32x4*)(lds + L_ST + 1024 + (m * 16 + tok) * 32 + 16);
      gc[m] = p0 + p1;
    }
    v4u wb[21];
    small_gemm<2048>(wa, lds + L_XH, lds + L_RT, w, l, a0, a1, a2, [&](auto k_) INL {
      constexpr int ks = decltype(k_)::v;
      if constexpr (ks < NPRE1) wb[ks] = load_gw(rs, O_W2, g, w, l, ks);
      if constexpr (ks == 4 || ks == 8) {
        constexpr int m = ks == 8;
        const float mean = (gc[m].x + gc[m].z) * (1.0f / D);
        const float var = fmaxf((gc[m].y + gc[m].w) * (1.0f / D) - mean * mean, 0.f);
        r1[m] = __builtin_amdgcn_rsqf(var + EPS); mr1[m] = -mean * r1[m];
        const float v2 = var * r1[m] * r1[m] + EPS, r2 = __builtin_amdgcn_rsqf(v2);
        gc[m] = gelu_consts(r1[m] * r2, (var + EPS) * r1[m] * v2 * r2);
      }
    });
    const int dl = g == 0 ? 1 : g == 1 ? -1 : g == 2 ? 2 : -2;
    int tt = t0 + prow + dl;
    const bool wrap = tt < 0 || tt >= S;
    tt &= S - 1;
    auto gl = [&](f32x4 v, f32x4 k) INL { return f32x4{gelu_ln(v.x, k), gelu_ln(v.y, k), gelu_ln(v.z, k), gelu_ln(v.w, k)}; };
    {
      v4u o = piece2(gl(a0, gc[0]), gl(a1, gc[1]));
      if (wrap) o = v4u{0, 0, 0, 0};
      stws(rs, o, frag_off(tt, pcp), O_HSH);
    }
    if (w < 2) {
      f32x4 t = gl(a2, w ? gc[1] : gc[0]);
      v4u o = piece2(t, t);
      if (wrap) o = v4u{0, 0, 0, 0};
      if ((q & 1) == w) stws(rs, o, frag_off(tt, pcp9), O_HSH);
    }
    SB();
#pragma unroll
    for (int i = NPRE1; i < 21; i++) wb[i] = load_gw(rs, O_W2, g, w, l, i);
    v4u tb[5];
    auto off2 = [&](int i) INL { return ldws(rs, min(tid + 512 * i, 2303) * 16, O_HSH + t32 * 36864); };
#pragma unroll
    for (int i = 0; i < 5; i++) tb[i] = off2(i);
    if (w == 7) fl = load_flags(rs, b, l);
    SB();
    auto xbinit = [&](int m, int col) INL {
      v2u hh = *(LAS v2u*)(lds + L_XH + frag_off(16 * m + tok, col >> 3) + (q & 1) * 8);
      const float r = m ? r1[1] : r1[0], mr = m ? mr1[1] : mr1[0];
      return f32x4{__builtin_fmaf(lo(hh.x), r, mr), __builtin_fmaf(hi(hh.x), r, mr), __builtin_fmaf(lo(hh.y), r, mr), __builtin_fmaf(hi(hh.y), r, mr)};
    };
    a0 = xbinit(0, 144 * g + 16 * w + 4 * q); a1 = xbinit(1, 144 * g + 16 * w + 4 * q);
    if (w < 2) a2 = xbinit(w, 144 * g + 128 + 4 * q);
    *(LAS v4u*)(lds + L_HS + w * 1024 + l * 16) = *(LAS v4u*)(lds + L_XH + frag_off(prow, pcp));
    if (w == 2) *(LAS v4u*)(lds + L_HS + 8192 + l * 16) = *(LAS v4u*)(lds + L_XH + frag_off(prow, pcp9));
    pin(a0); pin(a1); pin(a2);
    settle<21>(rs, wb, [&](int i) INL { return load_gw(rs, O_W2, g, w, l, i); },
               [&](int i, v4u v) INL { if (i >= KS) *(LAS v4u*)(lds + L_A9B + ks9(w, i - KS) * 1024 + l * 16) = v; }, alive);
    settle<5>(rs, tb, off2, [&](int i, v4u v) INL { *(LAS v4u*)(lds + L_B2 + min(tid + 512 * i, 2303) * 16) = v; }, alive);
    if (w == 7) wait_flags(rs, fl, b, l, epoch, 2, ~0ull, alive);
    bar();
    v4u ring[RW];
    const int sbase = O_WM + ((b & 7) * 1296 + (w < 4 ? w * 180 : 144 + w * 144)) * 1024;
    small_gemm<1024>(wb, lds + L_B2, lds + L_A9B, w, l, a0, a1, a2, [&](auto k_) INL {
      constexpr int ks = decltype(k_)::v;
      if constexpr (ks < NPRE2) ring[ks] = ring_load(rs, sbase, l * 16, ks);
    });
    stws(rs, piece2(a0, a1), frag_off(t0 + prow, pcp), O_X1);
    if (w < 2) { v4u o = piece2(a2, a2); if ((q & 1) == w) stws(rs, o, frag_off(t0 + prow, pcp9), O_X1); }
    SB();
#pragma unroll
    for (int i = NPRE2; i < RW; i++) ring[i] = ring_load(rs, sbase, l * 16, i);
    v4u tq[9];
    const int xo = ((l >> 3) & 3) * 256 + (8 * (w & 1) + (l & 7)) * 16;
    auto off3 = [&](int i) INL { return ldws(rs, (l >> 5) * 1024 + xo, O_X1 + ((4 * tile64 + (w >> 1)) * KS + 2 * i) * 1024); };
#pragma unroll
    for (int i = 0; i < 9; i++) tq[i] = off3(i);
    SB();
    epoch = __builtin_amdgcn_readfirstlane(*(LAS unsigned*)(lds + L_MISC + 16));
    settle<9>(rs, tq, off3, [&](int i, v4u v) INL { *(LAS v4u*)(lds + L_X + (8 * i + (w >> 1)) * 1024 + (l >> 5) * 4096 + xo) = v; }, alive);
    bar();
    if (w >= 4) {
      mlp_body<4>(lds, rs, ring, tq, sbase, b, w, epoch, fl);
#pragma unroll
      for (int i = 0; i < RW; i++) asm volatile("" : "=v"(ring[i]));
#pragma unroll
      for (int i = 0; i < 9; i++) asm volatile("" : "=v"(tq[i]));
    }
    int wv = w;
    asm volatile("" : "+s"(wv));
    if (wv < 4) mlp_body<5>(lds, rs, ring, tq, sbase, b, w, epoch, fl);
  }
  {
    const int l = lane(), tok = l & 15, q = l >> 4;
    const int pcp = 18 * g + 2 * w + (q >> 1), pcp9 = 18 * g + 16 + (q >> 1), prow = 16 * (q & 1) + tok;
    unsigned sel_lo = 0x3f80u, sel_hi = 0x3f800000u;
    asm volatile("" : "+s"(sel_lo), "+s"(sel_hi));
    wait_flags(rs, fl, b, l, epoch, 3, 3ull << (2 * tile64), alive);
    auto finish = [&](v4u (&pp)[8], int cp, int hs) INL {
      const v4u hh = *(LAS v4u*)(lds + L_HS + hs * 1024 + l * 16);
      const v4u xx = *(LAS v4u*)(lds + L_X + ((cp >> 2) * 4 + 2 * (t32 & 1) + (q & 1)) * 1024 + ((cp & 3) * 16 + tok) * 16);
      float s[8] = {lo(hh.x) + lo(xx.x), hi(hh.x) + hi(xx.x), lo(hh.y) + lo(xx.y), hi(hh.y) + hi(xx.y),
                    lo(hh.z) + lo(xx.z), hi(hh.z) + hi(xx.z), lo(hh.w) + lo(xx.w), hi(hh.w) + hi(xx.w)};
#pragma unroll
      for (int i = 0; i < 8; i++) pin(s[i]);
      const int po = frag_off(t0 + prow, cp);
      settle<8>(rs, pp, [&](int i) INL { return ldws(rs, po, O_PART + i * ACT); }, [&](int, v4u) INL {}, alive);
#pragma unroll
      for (int i = 0; i < 8; i++) {
        s[0] = dot2(pp[i].x, sel_lo, s[0]); s[1] = dot2(pp[i].x, sel_hi, s[1]); s[2] = dot2(pp[i].y, sel_lo, s[2]); s[3] = dot2(pp[i].y, sel_hi, s[3]);
        s[4] = dot2(pp[i].z, sel_lo, s[4]); s[5] = dot2(pp[i].z, sel_hi, s[5]); s[6] = dot2(pp[i].w, sel_lo, s[6]); s[7] = dot2(pp[i].w, sel_hi, s[7]);
      }
      *(v4u*)(out + (t0 + prow) * D + cp * 8) = v4u{pk(s[0], s[1]), pk(s[2], s[3]), pk(s[4], s[5]), pk(s[6], s[7])};
    };
    v4u pa[8], pc[8];
#pragma unroll
    for (int i = 0; i < 8; i++) pa[i] = ldws(rs, frag_off(t0 + prow, pcp), O_PART + i * ACT);
    if (w == 0) {
#pragma unroll
      for (int i = 0; i < 8; i++) pc[i] = ldws(rs, frag_off(t0 + prow, pcp9), O_PART + i * ACT);
    }
    SB();
    finish(pa, pcp, w);
    if (w == 0) finish(pc, pcp9, 8);
  }
}

extern "C" void kernel_launch(void* const* d_in, const int* in_sizes, int n_in, void* d_out, int out_size, void* d_ws, size_t ws_size,
                              hipStream_t stream) {
  mega<<<256, 512, 0, stream>>>((const u16*)d_in[0], (const u16*)d_in[1], (const u16*)d_in[2], (const u16*)d_in[3], (const u16*)d_in[4],
                                (const u16*)d_in[5], (const u16*)d_in[7], (const u16*)d_in[8], (const u16*)d_in[9], (const u16*)d_in[10],
                                (char*)d_ws, (u16*)d_out);
}
```

```cpp
#include <hip/hip_runtime.h>
#include <stdint.h>
typedef unsigned short u16;
typedef unsigned v4u __attribute__((ext_vector_type(4)));
typedef unsigned v2u __attribute__((ext_vector_type(2)));
typedef float f32x2 __attribute__((ext_vector_type(2)));
typedef float f32x4 __attribute__((ext_vector_type(4)));
typedef float f32x16 __attribute__((ext_vector_type(16)));
typedef __bf16 bf16x8 __attribute__((ext_vector_type(8)));
typedef __bf16 bf16x2 __attribute__((ext_vector_type(2)));
typedef short s16x4 __attribute__((ext_vector_type(4)));
typedef __amdgpu_buffer_rsrc_t rsrc_t;
#define LAS __attribute__((address_space(3)))
#define DEV __device__ __forceinline__
#define SB() __builtin_amdgcn_sched_barrier(0)
#define INL __attribute__((always_inline))
template <int V> struct IC { static constexpr int v = V; };
template <int I, int N, class Fn> DEV void sfor(Fn&& f) { if constexpr (I < N) { f(IC<I>{}); sfor<I + 1, N>(f); } }

constexpr int S = 2048, D = 576, F = 4608;
constexpr int KS = 18;
constexpr int ACT = S * D * 2;
constexpr int WIN = D * D * 2;
constexpr int O_EW = 0, O_FLAG = 1024, O_UT = 16384, O_YV = O_UT + 2 * ACT, O_W1 = O_YV + ACT, O_W2 = O_W1 + WIN, O_HSH = O_W2 + WIN,
              O_X1 = O_HSH + ACT, O_PART = O_X1 + ACT, O_WM = O_PART + 8 * ACT, WS_USED = O_WM + 16 * WIN;
constexpr float EPS = 1e-5f;
constexpr unsigned SENT = 0xffffffffu, EPOCH_STEP = 0x9E3779B9u, NSTAGE = 4;
constexpr int SPIN_CAP = 1 << 14;
constexpr int NCONV = 216;
#define NA 3
#define NB 5
#define NC 6
#define ND 6
#define W1S 6
#define WFL 3
#define W1E 16
#define SLP0 4
#define SLP1 0
#define SLP2 0
#define SLP3 0
#define SLP4 0
#define SLP5 0

constexpr int L_X = 0, L_H = 73728, L_HS = 147456, L_ST = 157760, L_CS = L_ST + 2048, L_MISC = L_CS + 1024, L_C9 = L_MISC + 64, L_BYTES = L_C9 + 512;
constexpr int L_XH = 0, L_RT = 36864;
constexpr int CU_SLOT = 95 * 80, CF_SLOT = 4192, CX_SLOT = 32 * 144;
constexpr int L_CU = L_H, L_CF = L_CU + 4 * CU_SLOT, L_CX = L_CF + 4 * CF_SLOT;
constexpr int L_Y = L_H, L_B2 = L_H, L_A9B = L_H + 36896;
static_assert(L_CX + 8 * CX_SLOT <= L_ST && L_BYTES <= 163840 && L_A9B <= L_CX && L_A9B + 18432 <= L_HS, "lds map");

DEV unsigned pk(float a, float b) { f32x2 v = {a, b}; return __builtin_bit_cast(unsigned, __builtin_convertvector(v, bf16x2)); }
DEV float lo(unsigned u) { return __builtin_bit_cast(float, u << 16); }
DEV float hi(unsigned u) { return __builtin_bit_cast(float, u & 0xffff0000u); }
DEV float asf(unsigned u) { return __builtin_bit_cast(float, u); }
DEV unsigned asu(float f) { return __builtin_bit_cast(unsigned, f); }
DEV float dot2(unsigned a, unsigned b, float c) {
  return __builtin_amdgcn_fdot2_f32_bf16(__builtin_bit_cast(bf16x2, a), __builtin_bit_cast(bf16x2, b), c, false);
}
DEV f32x4 mfma16(v4u a, v4u b, f32x4 c) {
  return __builtin_amdgcn_mfma_f32_16x16x32_bf16(__builtin_bit_cast(bf16x8, a), __builtin_bit_cast(bf16x8, b), c, 0, 0, 0);
}
DEV f32x16 mfma32(v4u a, v4u b, f32x16 c) {
  return __builtin_amdgcn_mfma_f32_32x32x16_bf16(__builtin_bit_cast(bf16x8, a), __builtin_bit_cast(bf16x8, b), c, 0, 0, 0);
}
DEV v2u trrd(LAS char* p) { return __builtin_bit_cast(v2u, __builtin_amdgcn_ds_read_tr16_b64_v4i16((LAS s16x4*)p)); }
DEV v4u ldws(rsrc_t rs, int voff, int soff) { return __builtin_amdgcn_raw_buffer_load_b128(rs, voff, soff, 16); }
DEV void stws(rsrc_t rs, v4u v, int voff, int soff) {
  __builtin_amdgcn_raw_buffer_store_b128(v, rs, voff, soff, 16);
  asm volatile("s_nop 1" :: "v"(v));
}
DEV void bar() { asm volatile("s_waitcnt lgkmcnt(0)\n\ts_barrier" ::: "memory"); }
DEV void drain() { asm volatile("s_waitcnt vmcnt(0)" ::: "memory"); }
DEV void swap16(unsigned& a, unsigned& b) { auto r = __builtin_amdgcn_permlane16_swap(a, b, false, false); unsigned x = r[0], y = r[1]; a = x; b = y; }
DEV void swap32(unsigned& a, unsigned& b) { auto r = __builtin_amdgcn_permlane32_swap(a, b, false, false); unsigned x = r[0], y = r[1]; a = x; b = y; }
DEV void glds16(const void* g, int ldsaddr) {
  unsigned keep;
  asm volatile("s_mov_b32 %0, m0\n\ts_mov_b32 m0, %2\n\ts_nop 0\n\tglobal_load_lds_dwordx4 %1, off\n\ts_mov_b32 m0, %0"
               : "=&s"(keep) : "v"(g), "s"(ldsaddr) : "memory");
}
DEV int lane() { int l; asm volatile("v_mbcnt_lo_u32_b32 %0, -1, 0\n\tv_mbcnt_hi_u32_b32 %0, -1, %0" : "=v"(l)); return l; }
template <class T> DEV void pin(T& v) { asm volatile("" : "+v"(v)); }
DEV unsigned mx4(unsigned m, v4u v) { m = max(max(m, v.x), v.y); return max(max(m, v.z), v.w); }
template <int N> DEV void vmwait() { asm volatile("s_waitcnt vmcnt(%0)" :: "n"(N) : "memory"); }
DEV float gelu_ln(float y, f32x4 c) {
  const float p = __builtin_fmaf(y * y, c.y, c.x);
  const float e = __builtin_amdgcn_exp2f(y * p);
  return y * __builtin_amdgcn_rcpf(__builtin_fmaf(e, c.z, c.z));
}
DEV f32x4 gelu_consts(float r, float rinv) {
  const float c0 = -2.0f * 0.7978845608028654f * 1.4426950408889634f, c1 = c0 * 0.044715f;
  return f32x4{r * c0, r * r * r * c1, rinv, 0.f};
}
DEV float stat_reduce(float s, float ss) {
  unsigned a = asu(s), b = asu(ss);
  swap16(a, b);
  float t = asf(a) + asf(b);
  unsigned c = asu(t), d = c;
  swap32(c, d);
  return asf(c) + asf(d);
}
DEV void stat_acc(v4u v, float& s, float& ss) {
  const unsigned one = 0x3f803f80u;
  s = dot2(v.x, one, s); ss = dot2(v.x, v.x, ss); s = dot2(v.y, one, s); ss = dot2(v.y, v.y, ss);
  s = dot2(v.z, one, s); ss = dot2(v.z, v.z, ss); s = dot2(v.w, one, s); ss = dot2(v.w, v.w, ss);
}
DEV int pairbase(int g) { return 144 * g + ((g & 1) << 4); }
DEV int singlebase(int g) { return 144 * g + ((g & 1) ? 0 : 128); }
DEV int frag_off(int row, int colpiece) { return (((row >> 4) * KS + (colpiece >> 2)) * 64 + (colpiece & 3) * 16 + (row & 15)) * 16; }

DEV v4u load_flags(rsrc_t rs, int b, int l) { return ldws(rs, l * 16, O_FLAG + (b & 7) * 1024); }
DEV void wait_flags(rsrc_t rs, v4u f, int b, int l, unsigned epoch, unsigned stage, uint64_t need, int& alive) {
  const unsigned base = epoch + stage, lim = NSTAGE - stage;
  auto bad = [&]() INL { return __builtin_amdgcn_ballot_w64(max(max(f.x - base, f.y - base), max(f.z - base, f.w - base)) > lim) & need; };
  uint64_t any = bad();
#pragma nounroll
  while (any != 0 && alive > 0) {
    asm volatile("s_sleep 1" ::: "memory");
    f = load_flags(rs, b, l);
    --alive;
    any = bad();
  }
  asm volatile("" ::: "memory");
}
DEV void publish_flag(rsrc_t rs, int b, int l, unsigned val) {
  if (l < 8) __builtin_amdgcn_raw_buffer_store_b32(val, rs, l * 1024 + b * 4, O_FLAG, 16);
}
template <int N, class OFF, class USE>
DEV void settle(rsrc_t rs, v4u (&d)[N], OFF off, USE use, int& alive) {
  unsigned m = 0;
#pragma unroll
  for (int i = 0; i < N; i++) { m = mx4(m, d[i]); use(i, d[i]); }
  uint64_t any = __builtin_amdgcn_ballot_w64(m == SENT);
#pragma nounroll
  while (any != 0 && alive > 0) {
    asm volatile("" ::: "memory");
    if (m == SENT) {
#pragma unroll
      for (int i = 0; i < N; i++) d[i] = off(i);
      m = 0;
#pragma unroll
      for (int i = 0; i < N; i++) { m = mx4(m, d[i]); use(i, d[i]); }
    }
    --alive;
    any = __builtin_amdgcn_ballot_w64(m == SENT);
  }
}

template <int N, class OFF, class USE>
DEV void settle_tag(rsrc_t rs, v4u (&d)[N], OFF off, USE use, unsigned tag, int& alive) {
  unsigned m = 0;
#pragma unroll
  for (int i = 0; i < N; i++) { m |= (d[i].z ^ tag) | (d[i].w ^ ~tag); use(i, d[i]); }
  uint64_t any = __builtin_amdgcn_ballot_w64(m != 0);
#pragma nounroll
  while (any != 0 && alive > 0) {
    asm volatile("" ::: "memory");
    if (m != 0) {
#pragma unroll
      for (int i = 0; i < N; i++) d[i] = off(i);
      m = 0;
#pragma unroll
      for (int i = 0; i < N; i++) { m |= (d[i].z ^ tag) | (d[i].w ^ ~tag); use(i, d[i]); }
    }
    --alive;
    any = __builtin_amdgcn_ballot_w64(m != 0);
  }
}

struct Job { int kind, j, s; };
DEV Job job_main(int n) { Job jb; jb.kind = 2 + (n >= 144); n -= (n >= 144) * 144; jb.j = n & 7; jb.s = n >> 3; return jb; }
DEV int slot_ks(int s) {
  if (s == 12) return 4;
  if (s == 13) return 13;
  const int g = (s < 14 ? s : s - 14) & 3, k0 = pairbase(g) >> 5;
  return k0 + (s < 4 ? 0 : s < 8 ? 2 : s < 12 ? 1 : 3);
}
struct WPtr { const u16 *w1, *w2, *w3, *w4; };
DEV void retile_load(Job jb, int stage, int r0, int rstep, int n, int l, const WPtr& wp) {
  const u16* src; int ld = D;
  if (jb.kind == 2) { src = wp.w3 + D * jb.j + 32 * slot_ks(jb.s); ld = F; }
  else src = wp.w4 + D * jb.j * D + 32 * slot_ks(jb.s);
  src += (l >> 2) * ld + (l & 3) * 8;
#pragma unroll
  for (int it = 0; it < 9; it++) {
    const int r = r0 + rstep * it;
    if (it < n && r < KS) {
      const u16* p = src + 32 * (jb.kind == 3 ? slot_ks(r) : r) * ld;
      glds16(p, stage + r * 2048); glds16(p + 16 * ld, stage + r * 2048 + 1024);
    }
  }
}
DEV void retile_read(Job jb, LAS char* lds, int stage, int w, int l, v4u (&fr)[3][2]) {
  const int q = l >> 4, a = (l & 15) >> 2, bb = l & 3;
  const bool nat = jb.s == 12 || jb.s == 13;
  LAS char* p = lds + stage + w * 2048 + (8 * q + a) * 64 + (nat ? 8 * bb : 16 * bb);
  const int ts = nat ? 32 : 8;
#pragma unroll
  for (int it = 0; it < 3; it++)
#pragma unroll
    for (int t = 0; t < 2; t++) {
      if (it < 2 || w < 2) { v2u x0 = trrd(p + it * 16384 + t * ts), x1 = trrd(p + it * 16384 + t * ts + 256); fr[it][t] = v4u{x0.x, x0.y, x1.x, x1.y}; }
      else fr[it][t] = v4u{0, 0, 0, 0};
    }
  if (!(jb.kind & 1)) {
    const v4u ones = {0x3f803f80u, 0x3f803f80u, 0x3f803f80u, 0x3f803f80u};
#pragma unroll
    for (int t = 0; t < 2; t++) {
      f32x4 c = {0, 0, 0, 0};
#pragma unroll
      for (int it = 0; it < 3; it++) c = mfma16(ones, fr[it][t], c);
      *(LAS float*)(lds + L_CS + (t * 16 + (l & 15)) * 32 + w * 4) = c.x;
    }
  }
}
DEV void retile_emit(Job jb, LAS char* lds, rsrc_t rs, int w, int l, v4u (&fr)[3][2]) {
  if (!(jb.kind & 1)) {
#pragma unroll
    for (int t = 0; t < 2; t++) {
      const f32x4 p0 = *(LAS f32x4*)(lds + L_CS + (t * 16 + (l & 15)) * 32), p1 = *(LAS f32x4*)(lds + L_CS + (t * 16 + (l & 15)) * 32 + 16);
      const float mu = (((p0.x + p0.y) + (p0.z + p0.w)) + ((p1.x + p1.y) + (p1.z + p1.w))) * (1.0f / D);
#pragma unroll
      for (int it = 0; it < 3; it++) if (it < 2 || w < 2) {
        const unsigned c0 = fr[it][t].x, c1 = fr[it][t].y, c2 = fr[it][t].z, c3 = fr[it][t].w;
        fr[it][t] = v4u{pk(lo(c0) - mu, hi(c0) - mu), pk(lo(c1) - mu, hi(c1) - mu), pk(lo(c2) - mu, hi(c2) - mu), pk(lo(c3) - mu, hi(c3) - mu)};
      }
    }
  }
  const int s = jb.s;
#pragma unroll
  for (int t = 0; t < 2; t++) {
    const int ww = s < 8 ? s : s < 12 ? s - 8 : s < 14 ? 2 * (s - 12) + t : s - 10, ti = s < 8 ? t : (s == 12 || s == 13) ? 4 : 2 + t;
    const int nt = ww < 4 ? 5 : 4, bs = jb.j * 1296 + (ww < 4 ? ww * 180 : 144 + ww * 144);
#pragma unroll
    for (int it = 0; it < 3; it++) if (it < 2 || w < 2) {
      const int r = w + 8 * it;
      const int c0 = jb.kind == 2 ? 0 : 32 * nt, c1 = jb.kind == 2 ? 36 : 32 * nt + 8, rr = jb.kind == 2 ? r : r - 14;
      const int f = rr < 0 ? 18 * nt + r * nt + ti : ti < 2 ? c0 + rr * 2 + ti : c1 + rr * (nt - 2) + ti - 2;
      stws(rs, fr[it][t], l * 16, O_WM + (bs + f) * 1024);
    }
  }
}

DEV void unit_load(const u16* wsrc, int u, int stage, int l) {
  const int row = l >> 3, sw = (row & 2);
  const u16* p = wsrc + (32 * (u / 9) + row) * D + 64 * (u % 9);
#pragma unroll
  for (int i = 0; i < 4; i++) glds16(p + 8 * i * D + (((l & 7) ^ sw ^ ((i & 1) << 2)) << 3), stage + i * 1024);
}
DEV void unit_emit(LAS char* st, rsrc_t rs, int base, int u, int l) {
  const int q = l >> 4, a = (l & 15) >> 2, bb = l & 3, sw = (a & 2) | ((q & 1) << 2);
#pragma unroll
  for (int t = 0; t < 4; t++) {
    LAS char* p = st + (8 * q + a) * 128 + (((2 * t + (bb >> 1)) ^ sw) << 4) + (bb & 1) * 8;
    const v2u x0 = trrd(p), x1 = trrd(p + 512);
    stws(rs, v4u{x0.x, x0.y, x1.x, x1.y}, l * 16, base + ((4 * (u % 9) + t) * KS + u / 9) * 1024);
  }
}

template <bool NINE, int A9S, class SIDE>
DEV void small_gemm_t(const v4u (&wa)[21], LAS char* p0, LAS char* p1, LAS char* a9, f32x4& a0, f32x4& a1, f32x4& a2, SIDE side) {
  v4u b0[3], b1[3], wn[3];
  auto rd = [&](int ks) INL {
    b0[ks % 3] = *(LAS v4u*)(p0 + ks * 1024); b1[ks % 3] = *(LAS v4u*)(p1 + ks * 1024);
    if (NINE) wn[ks % 3] = *(LAS v4u*)(a9 + ks * A9S);
  };
  rd(0); rd(1);
  SB();
  sfor<0, KS>([&](auto k_) INL {
    constexpr int ks = decltype(k_)::v;
    if constexpr (ks + 2 < KS) rd(ks + 2);
    a0 = mfma16(wa[ks], b0[ks % 3], a0); a1 = mfma16(wa[ks], b1[ks % 3], a1);
    if constexpr (NINE) a2 = mfma16(wn[ks % 3], b0[ks % 3], a2);
    side(k_);
    SB();
  });
}
template <int A9S, class SIDE>
DEV void small_gemm(const v4u (&wa)[21], LAS char* bimg, LAS char* a9, int w, int l, f32x4& a0, f32x4& a1, f32x4& a2, SIDE side) {
  LAS char* p = bimg + l * 16;
  if (w < 2) {
    f32x4 x = w ? a1 : a0, y = w ? a0 : a1;
    small_gemm_t<true, A9S>(wa, p + w * KS * 1024, p + (w ^ 1) * KS * 1024, a9 + l * 16, x, y, a2, side);
    a0 = w ? y : x; a1 = w ? x : y;
  } else small_gemm_t<false, A9S>(wa, p, p + KS * 1024, a9, a0, a1, a2, side);
}
DEV int ks9(int w, int i) { return i < 2 || w < 2 ? w + 8 * i : w + 8; }
DEV v4u load_gw(rsrc_t rs, int base, int g, int w, int l, int i) {
  return i < KS ? ldws(rs, l * 16 + (i & 3) * 1024, base + ((9 * g + w) * KS + (i & ~3)) * 1024)
                : ldws(rs, l * 16, base + ((9 * g + 8) * KS + ks9(w, i - KS)) * 1024);
}
DEV v4u piece2(f32x4 a, f32x4 b) {
  unsigned a0 = pk(a.x, a.y), a1 = pk(a.z, a.w), b0 = pk(b.x, b.y), b1 = pk(b.z, b.w);
  swap16(a0, b0); swap16(a1, b1);
  return v4u{a0, a1, b0, b1};
}

template <int NT> struct Seg {
  static constexpr int steps(int s) { return s < 2 ? 18 : s == 2 ? 8 : s == 3 ? 6 : 4; }
  static constexpr int nt(int s) { return s == 0 || s == 4 ? 2 : s == 1 || s == 5 ? NT - 2 : NT; }
  static constexpr int base(int s) { int b = 0; for (int i = 0; i < s; i++) b += steps(i) * nt(i); return b; }
  static constexpr int slot0(int s) { return s < 3 ? 0 : s == 3 ? 8 : 14; }
};
constexpr int RW = 16;
DEV v4u ring_load(rsrc_t rs, int sbase, int l16, int f) { return ldws(rs, l16 + (f & 3) * 1024, sbase + (f & ~3) * 1024); }

template <int NT>
DEV void mlp_body(LAS char* lds, rsrc_t rs, v4u (&ring)[RW], const v4u (&tx)[9], int sbase, int b, int w, unsigned epoch, v4u& fl
) {
  using G = Seg<NT>;
  constexpr int TOT = 36 * NT;
  const int g = w & 3, odd = g & 1, j = b & 7, tile = b >> 3;
  const int l16 = lane() * 16;
  LAS char* const bx = lds + L_X + l16;
  LAS char* const bh = lds + L_H + l16;
  auto bptr = [&](int s, int st) INL { return (s < 2 ? bx : bh) + (G::slot0(s) + st) * 4096; };
  f32x4 acc1[NT][4], acc2[NT][4];
#pragma unroll
  for (int i = 0; i < NT; i++)
#pragma unroll
    for (int m = 0; m < 4; m++) { acc1[i][m] = f32x4{0, 0, 0, 0}; acc2[i][m] = f32x4{0, 0, 0, 0}; }
  v4u bf[4];
#pragma unroll
  for (int m = 0; m < 4; m++) bf[m] = *(LAS v4u*)(bx + m * 1024);
  float t0 = 0, t1 = 0, sa = 0, sb = 0, qa = 0, qb = 0;
  f32x4 gc[2];
  unsigned go[4];
  LAS char *gcp = lds, *hp = lds, *hs = lds;
  const v4u sent = {SENT, SENT, SENT, SENT};
  auto gelu_chunk = [&](auto c_, auto tb_, auto per_) INL {
    constexpr int c = decltype(c_)::v, tb = decltype(tb_)::v, PER = decltype(per_)::v, P = c / 10, k = c % 10, m = P / PER, e = 2 * (P % PER);
    const f32x4 av = acc1[e < 8 ? tb + (e >> 2) : NT - 1][m], kc = gc[m & 1];
    const float y0 = av[e & 3], y1 = av[(e & 3) + 1];
    if constexpr (k == 0) { t0 = y0 * y0; pin(t0); t1 = y1 * y1; }
    if constexpr (k == 1) { t0 = __builtin_fmaf(t0, kc.y, kc.x); pin(t0); t1 = __builtin_fmaf(t1, kc.y, kc.x); }
    if constexpr (k == 2 || k == 8) { t0 *= y0; pin(t0); t1 *= y1; }
    if constexpr (k == 3) t0 = __builtin_amdgcn_exp2f(t0);
    if constexpr (k == 4) t1 = __builtin_amdgcn_exp2f(t1);
    if constexpr (k == 5) { t0 = __builtin_fmaf(t0, kc.z, kc.z); pin(t0); t1 = __builtin_fmaf(t1, kc.z, kc.z); }
    if constexpr (k == 6) t0 = __builtin_amdgcn_rcpf(t0);
    if constexpr (k == 7) t1 = __builtin_amdgcn_rcpf(t1);
    if constexpr (k == 9) {
      go[(e >> 1) & 3] = pk(t0, t1);
      if constexpr (e == 6) *(LAS v4u*)(hp + m * 1024) = v4u{go[0], go[1], go[2], go[3]};
      if constexpr (e == 10) *(LAS v2u*)(hs + m * 1024) = v2u{go[0], go[1]};
      if constexpr (e == 0 && m < 3) gc[(m + 1) & 1] = *(LAS f32x4*)(gcp + (m + 1) * 256);
    }
  };
  auto gelu_setup = [&](int slot, bool single) INL {
    gcp = lds + L_ST + (l16 & 255);
    gc[0] = *(LAS f32x4*)gcp;
    hp = bh + slot * 4096;
    if (single) hs = lds + L_H + (12 + (g >> 1)) * 4096 + (2 * odd + (l16 >> 9)) * 256 + (l16 & 255) + ((l16 >> 5) & 8);
  };
  auto stat_chunk = [&](auto c_) INL {
    constexpr int c = decltype(c_)::v, d = c / 3, k = c % 3;
    if constexpr (k == 0) { const unsigned v = tx[d >> 2][d & 3]; t0 = lo(v); t1 = hi(v); }
    if constexpr (k == 1) { sa += t0; pin(sa); sb += t1; }
    if constexpr (k == 2) { qa = __builtin_fmaf(t0, t0, qa); pin(qa); qb = __builtin_fmaf(t1, t1, qb); }
  };
  const int pdst = O_PART + j * ACT + 4 * tile * KS * 1024, ks0 = (pairbase(g) >> 5) + (NT == 5 ? 0 : 2);
  auto store_pair = [&](int pp, int m) INL {
    f32x4 a = acc2[2 * pp][m], c = acc2[2 * pp + 1][m];
    stws(rs, v4u{pk(a.x, a.y), pk(a.z, a.w), pk(c.x, c.y), pk(c.z, c.w)}, l16, pdst + (m * KS + ks0 + pp) * 1024);
  };
  sfor<0, 6>([&](auto s_) INL {
    constexpr int s = decltype(s_)::v, nst = G::steps(s), n = G::nt(s), tb = (s == 1 || s == 5) ? 2 : 0;
    constexpr int NCH = s == 0 ? 108 : s == 1 ? 160 : s == 2 && NT == 5 ? 240 : s == 3 && NT == 4 ? 160 : 0;
    constexpr int SPAN = s == 0 ? 116 : (nst - 1) * 4 * n - 2;
    sfor<0, nst>([&](auto st_c) INL {
      constexpr int st = decltype(st_c)::v;
      constexpr bool last = st == nst - 1;
      if constexpr (last && s == 1) vmwait<RW>();
      if constexpr (last && s < 4) bar();
      if constexpr (last && s == 1 && NT == 4) { if (w == 7) publish_flag(rs, b, l16 >> 4, epoch + 4); }
      if constexpr (last && s == 0) gelu_setup(w, false);
      if constexpr (last && s == (NT == 5 ? 1 : 2)) gelu_setup(NT == 5 ? 8 + g : 14 + g, NT == 5);
      constexpr int f0 = G::base(s) + st * n;
      LAS char* np = last ? (s < 5 ? bptr(s + 1, 0) : lds) : bptr(s, st + 1);
      sfor<0, 4 * n>([&](auto k_) INL {
        constexpr int k = decltype(k_)::v, m = k / n, i = k % n, u = 4 * n * st + k, f = f0 + i;
        if constexpr (s < 2) acc1[tb + i][m] = mfma16(ring[f % RW], bf[m], acc1[tb + i][m]);
        else acc2[tb + i][m] = mfma16(ring[f % RW], bf[m], acc2[tb + i][m]);
        if constexpr (i == n - 1 && !(last && s == 5)) bf[m] = *(LAS v4u*)(np + m * 1024);
        if constexpr (m == 3 && f + RW < TOT) ring[f % RW] = ring_load(rs, sbase, l16, f + RW);
        if constexpr (NCH > 0 && u < SPAN) sfor<u * NCH / SPAN, (u + 1) * NCH / SPAN>([&](auto c_) INL {
          if constexpr (s == 0) stat_chunk(c_);
          else gelu_chunk(c_, IC<(s == 1 ? 0 : 2)>{}, IC<(s == 2 ? 6 : 4)>{});
        });
        if constexpr (s == 0 && u == 118) {
          sa += sb; qa += qb;
          sa += asf(__builtin_amdgcn_update_dpp(0, asu(sa), 0x128, 0xf, 0xf, false));
          qa += asf(__builtin_amdgcn_update_dpp(0, asu(qa), 0x128, 0xf, 0xf, false));
        }
        if constexpr (s == 0 && u == 120) sa = stat_reduce(sa, qa);
        if constexpr (s == 0 && u == 123) {
          unsigned a = asu(sa), c = a;
          swap16(a, c);
          const float mean = asf(a) * (1.0f / D);
          qa = fmaxf(asf(c) * (1.0f / D) - mean * mean, 0.f) + EPS;
        }
        if constexpr (s == 0 && u == 126) { const float r = __builtin_amdgcn_rsqf(qa); *(LAS f32x4*)(lds + L_ST + w * 128 + (l16 & 127)) = gelu_consts(r, qa * r); }
        if constexpr (s < 2 && k == 1 && (18 * s + st) % 3 == 1 && 18 * s + st < 27) stws(rs, sent, l16, pdst + (w * 9 + (18 * s + st) / 3) * 1024);
        if constexpr (s == 4 && u == 0) fl = load_flags(rs, b, l16 >> 4);
        if constexpr (s == 5 && st == 0 && i == 0) store_pair(0, m);
        if constexpr (s == 5 && last && m > 0 && i == n - 1) store_pair(1, m - 1);
        SB();
      });
    });
  });
  store_pair(1, 3);
  if (NT == 5) {
    const int l = lane(), q = l >> 4, cp = (singlebase(g) >> 3) + (q >> 1);
#pragma unroll
    for (int m = 0; m < 4; m += 2) stws(rs, piece2(acc2[4][m], acc2[4][m + 1]), frag_off(16 * (m + (q & 1)) + (l & 15), cp), pdst);
  }
}

__global__ __launch_bounds__(512, 2) void mega(const u16* __restrict__ x, const u16* __restrict__ kin, const u16* __restrict__ vin,
                                               const u16* __restrict__ sk, const u16* __restrict__ filt, const u16* __restrict__ Dv,
                                               const u16* __restrict__ w1, const u16* __restrict__ w2, const u16* __restrict__ w3,
                                               const u16* __restrict__ w4, char* ws, u16* out) {
  __shared__ __attribute__((aligned(16))) char smem[L_BYTES];
  LAS char* lds = (LAS char*)smem;
  const int ldsb = (int)(uintptr_t)lds;
  const int tid = threadIdx.x, l = tid & 63, w = __builtin_amdgcn_readfirstlane(tid >> 6), b = blockIdx.x;
  const rsrc_t rs = __builtin_amdgcn_make_buffer_rsrc((void*)ws, 0, WS_USED, 0x00020000);
  const v4u sent = {SENT, SENT, SENT, SENT};
  asm volatile("" :: "s"(x), "s"(kin), "s"(vin), "s"(sk), "s"(filt), "s"(Dv), "s"(w1), "s"(w2), "s"(w3), "s"(w4), "s"(ws), "s"(out));
  const WPtr wp = {w1, w2, w3, w4};
  const bool conv = b < NCONV;
  const int tok = l & 15, q = l >> 4;
  int alive = SPIN_CAP;

  const int xcd = b & 7, rk = b >> 3;
  const int ci = 27 * xcd + rk, tb8 = 32 * xcd + rk;
  const int pb = 8 * (ci / 3) + 2 * (ci % 3);
  auto chan = [&](int p) INL { return p < 288 ? 2 * p : 2 * (p - 288) + 1; };
  const int tile64 = rk, g = rk & 3, t32 = 8 * xcd + (rk >> 2), t0 = 32 * t32;
  const int rt = w & 1, kq = w >> 1;
  const Job jm = job_main(b);
  auto stage_x = [&](int n0, int nstep, int cnt) INL {
#pragma unroll
    for (int it = 0; it < 18; it++) {
      const int n = n0 + nstep * it;
      if (it < cnt && n < 36) glds16(x + (t0 + 8 * (n / 9) + (l >> 3)) * D + 64 * (n % 9) + (((l & 7) ^ ((l >> 3) & 6)) << 3), ldsb + L_XH + n * 1024);
    }
  };
  const int part = ci % 3, u24 = 8 * part + w, c8 = u24 / 3, r3 = u24 % 3;
  const int type = c8 == 2 ? (r3 + 1) % 3 : r3;
  const int slot = c8 - 2 * part, nt = type != 0, cb = type == 2 ? 32 : 0;
  const int yo = (l >> 2) * D * 64 + (l & 3) * 16, ys = O_YV + ((32 * nt + (type == 2 ? 16 : 0)) * D + pb + slot) * 64;
  unsigned epoch = 0;
  {
    unsigned kk[11], vv[8], dv = 0; v4u sw;
    const int cp = min(tid, 287);
    if (w < 5) {
      const unsigned* kp = (const unsigned*)(kin + (8 * tb8 - 3) * D) + cp;
      const unsigned* vp = (const unsigned*)(vin + 8 * tb8 * D) + cp;
#pragma unroll
      for (int i = 0; i < 11; i++) kk[i] = kp[(i < 3 && tb8 == 0 ? 3 : i) * (D / 2)];
#pragma unroll
      for (int i = 0; i < 8; i++) vv[i] = vp[i * (D / 2)];
      sw = *(const v4u*)(sk + cp * 8);
      epoch = __builtin_amdgcn_raw_buffer_load_b32(rs, 0, O_EW, 16);
      dv = Dv[min(chan(pb + (l & 3)), D - 1)];
    }
    asm volatile("s_barrier" ::: "memory");
    if (w == 5 && b < 162) unit_load(w1, b, ldsb + L_CX, l);
    if (w == 6 && b >= 94) unit_load(w2, b - 94, ldsb + L_CX + 4096, l);
    if (w == 5) epoch = __builtin_amdgcn_raw_buffer_load_b32(rs, 0, O_EW, 16);
    if (w >= 6) {
      if (conv) {
#pragma unroll
        for (int i = 0; i < 10; i++) {
          const int n = w == 6 ? i : 6 + i, sl = n >> 2;
          if (i < 6 || w == 7) glds16(filt + chan(pb + sl) * S + ((n & 3) * 64 + l) * 8, ldsb + L_CF + sl * CF_SLOT + 80 + (n & 3) * 1024);
        }
      }
    }
    if (conv) {
      stws(rs, sent, yo, ys);
      if (type == 0) stws(rs, sent, yo, ys + 16 * D * 64);
      for (int i = tid; i < 4 * 155; i += 512) *(LAS v4u*)(lds + L_CU + (i / 155) * CU_SLOT + (i % 155) * 16) = v4u{0, 0, 0, 0};
      if (tid < 24) { const int sl = tid / 6, pc = tid % 6; *(LAS v4u*)(lds + L_CF + sl * CF_SLOT + (pc < 5 ? pc * 16 : 80 + 4096)) = v4u{0, 0, 0, 0}; }
    }
    if (w < 5) {
      pin(epoch); pin(dv);
      epoch = __builtin_amdgcn_readfirstlane(epoch) + EPOCH_STEP;
      if (tb8 == 0) kk[0] = kk[1] = kk[2] = 0;
      float u0[8], u1[8];
      unsigned ql[11], qh[11];
#pragma unroll
      for (int i = 1; i < 11; i++) { ql[i] = __builtin_amdgcn_perm(kk[i - 1], kk[i], 0x05040100u); qh[i] = __builtin_amdgcn_perm(kk[i - 1], kk[i], 0x07060302u); }
#pragma unroll
      for (int r = 0; r < 8; r++) {
        u0[r] = dot2(ql[r + 3], sw.x, dot2(ql[r + 1], sw.y, 0.f)) * lo(vv[r]);
        u1[r] = dot2(qh[r + 3], sw.z, dot2(qh[r + 1], sw.w, 0.f)) * hi(vv[r]);
      }
      if (tid < 288) {
        const int uo = O_UT + tb8 * 18432;
        stws(rs, v4u{pk(u0[7], u0[6]), pk(u0[5], u0[4]), epoch, ~epoch}, cp * 16, uo);
        stws(rs, v4u{pk(u1[7], u1[6]), pk(u1[5], u1[4]), epoch, ~epoch}, cp * 16, uo + 4608);
        stws(rs, v4u{pk(u0[3], u0[2]), pk(u0[1], u0[0]), epoch, ~epoch}, cp * 16, uo + 9216);
        stws(rs, v4u{pk(u1[3], u1[2]), pk(u1[1], u1[0]), epoch, ~epoch}, cp * 16, uo + 13824);
      }
      if (w == 4) { *(LAS unsigned*)(lds + L_MISC + (l & 3) * 4) = dv << 16; *(LAS unsigned*)(lds + L_MISC + 16) = epoch; }
    } else if (w == 5) {
      drain();
      if (b < 162) unit_emit(lds + L_CX, rs, O_W1, b, l);
    }
    bar();
    if (w == 5) {
      drain();
      publish_flag(rs, b, l, epoch + EPOCH_STEP + 1);
    } else if (w == 6 && b >= 94) {
      drain();
      unit_emit(lds + L_CX + 4096, rs, O_W2, b - 94, l);
    }
  }

  v4u wa[21], fl = sent;
  if (conv) {
    const int lam = l & 31, h = l >> 5;
    if (w < 4) {
      __builtin_amdgcn_s_sleep(SLP1);
      v4u tu[8];
      auto off = [&](int i) INL { return ldws(rs, ((tid >> 2) * D + (tid & 3)) * 16, O_UT + (i * 64 * D + pb) * 16); };
#pragma unroll
      for (int i = 0; i < 8; i++) tu[i] = off(i);
      settle_tag<8>(rs, tu, off, [&](int i, v4u v) INL { *(LAS v2u*)(lds + L_CU + (tid & 3) * CU_SLOT + (31 + 8 * i + (tid >> 5)) * 80 + ((tid >> 2) & 7) * 8) = v2u{v.x, v.y}; }, epoch, alive);
      drain();
    }
    else drain();
    bar();
    const int e0 = 32 * (cb - 1 + 16 * h) + lam + 33;
    LAS unsigned* fp = (LAS unsigned*)(lds + L_CF + slot * CF_SLOT) + (e0 >> 1);
    const unsigned sh = (e0 & 1) * 16;
    LAS char* ub = lds + L_CU + slot * CU_SLOT + (31 + 32 * nt + lam - cb - 16 * h - 15) * 80;
    unsigned dd[2][5];
    v4u R[2], bq[2][4];
    auto rdf = [&](int m) INL {
#pragma unroll
      for (int i = 0; i < 5; i++) dd[m & 1][i] = fp[16 * m + i];
    };
    auto mkr = [&](int m) INL {
      const unsigned* d = dd[m & 1];
      R[m & 1] = v4u{__builtin_amdgcn_alignbit(d[1], d[0], sh), __builtin_amdgcn_alignbit(d[2], d[1], sh), __builtin_amdgcn_alignbit(d[3], d[2], sh), __builtin_amdgcn_alignbit(d[4], d[3], sh)};
    };
    auto rdb = [&](int m) INL {
#pragma unroll
      for (int s = 0; s < 4; s++) bq[m & 1][s] = *(LAS v4u*)(ub + (15 - m) * 80 + 16 * s);
    };
    rdf(0); rdf(1); rdb(0);
    v2u uu[4];
#pragma unroll
    for (int o = 0; o < 4; o++) uu[o] = *(LAS v2u*)(lds + L_CU + slot * CU_SLOT + (31 + 32 * nt + lam) * 80 + 16 * o + (h ? 0 : 8));
    const float dcoef = type == 2 ? 0.f : *(LAS float*)(lds + L_MISC + slot * 4);
    epoch = __builtin_amdgcn_readfirstlane(*(LAS unsigned*)(lds + L_MISC + 16));
    if (w == 5) publish_flag(rs, b, l, epoch + 2);
    if (b < 192) {
#pragma unroll
      for (int i = 0; i < 3; i++) stws(rs, sent, l * 16 + i * 1024, O_HSH + (b * 8 + w) * 3072);
    }
    retile_load(jm, ldsb + L_RT, w, 8, 3, l, wp);
    stage_x(w, 8, 5);
    f32x16 acc[4];
#pragma unroll
    for (int s = 1; s < 4; s++)
#pragma unroll
      for (int i = 0; i < 16; i++) acc[s][i] = 0.f;
#pragma unroll
    for (int o = 0; o < 4; o++) { acc[0][4 * o] = hi(uu[o].y) * dcoef; acc[0][4 * o + 1] = lo(uu[o].y) * dcoef; acc[0][4 * o + 2] = hi(uu[o].x) * dcoef; acc[0][4 * o + 3] = lo(uu[o].x) * dcoef; }
    mkr(0);
    SB();
    sfor<0, 16>([&](auto m_) INL {
      constexpr int m = decltype(m_)::v;
      mkr(m + 1);
      if constexpr (m < 15) { rdf(m + 2); rdb(m + 1); }
      acc[0] = mfma32(R[(m + 1) & 1], bq[m & 1][0], acc[0]);
#pragma unroll
      for (int s = 1; s < 4; s++) {
        const bool c = lam < 32 - 8 * s;
        const v4u A = R[(m + 1) & 1], Z = R[m & 1];
        acc[s] = mfma32(v4u{c ? A.x : Z.x, c ? A.y : Z.y, c ? A.z : Z.z, c ? A.w : Z.w}, bq[m & 1][s], acc[s]);
      }
      if constexpr (m == W1S - WFL) fl = load_flags(rs, b, l);
      if constexpr (m == W1S) wait_flags(rs, fl, b, l, epoch, 1, (1ull << 41) - 1, alive);
      if constexpr (m >= W1S && m < W1E) {
#pragma unroll
        for (int i = 21 * (m - W1S) / (W1E - W1S); i < 21 * (m + 1 - W1S) / (W1E - W1S); i++) wa[i] = load_gw(rs, O_W1, g, w, l, i);
      }
      SB();
    });
#pragma unroll
    for (int o = 0; o < 4; o++) {
      f32x4 y;
#pragma unroll
      for (int e = 0; e < 4; e++) { const int i = 4 * o + e; y[e] = (acc[0][i] + acc[1][(i + 12) & 15]) + (acc[2][(i + 8) & 15] + acc[3][(i + 4) & 15]); }
      *(LAS f32x4*)(lds + L_CX + w * CX_SLOT + lam * 144 + 32 * o + 16 * h) = y;
    }
    vmwait<21>();
    if (w == 4) fl = load_flags(rs, b, l);
    bar();
    LAS char* cx = lds + L_CX + (l >> 2) * 144 + (l & 3) * 32;
    if (type == 0) {
#pragma unroll
      for (int i = 0; i < 2; i++) {
        const f32x4 a = *(LAS f32x4*)(cx + w * CX_SLOT + i * 2304), c = *(LAS f32x4*)(cx + w * CX_SLOT + i * 2304 + 16);
        stws(rs, v4u{pk(a.x, a.y), pk(a.z, a.w), pk(c.x, c.y), pk(c.z, c.w)}, yo, ys + i * 16 * D * 64);
      }
    } else {
      LAS char* p = cx + (w + 1 - type) * CX_SLOT + (type - 1) * 2304;
      const f32x4 a = *(LAS f32x4*)p + *(LAS f32x4*)(p + CX_SLOT), c = *(LAS f32x4*)(p + 16) + *(LAS f32x4*)(p + CX_SLOT + 16);
      stws(rs, v4u{pk(a.x, a.y), pk(a.z, a.w), pk(c.x, c.y), pk(c.z, c.w)}, yo, ys);
    }
  } else {
    v4u fr[3][2];
    const Job jx = job_main(b + 40);
    drain();
    if (b < NCONV + 32) retile_load(jx, ldsb + L_H, w, 8, 3, l, wp);
    bar();
    epoch = __builtin_amdgcn_readfirstlane(*(LAS unsigned*)(lds + L_MISC + 16));
    if (w == 5) publish_flag(rs, b, l, epoch + 2);
    retile_load(jm, ldsb + L_RT, w, 8, 3, l, wp);
    stage_x(w, 8, 5);
    if (b < NCONV + 32) {
      vmwait<8>();
      retile_read(jx, lds, L_H, w, l, fr);
      retile_emit(jx, lds, rs, w, l, fr);
    }
    wait_flags(rs, load_flags(rs, b, l), b, l, epoch, 1, (1ull << 41) - 1, alive);
#pragma unroll
    for (int i = 0; i < 21; i++) wa[i] = load_gw(rs, O_W1, g, w, l, i);
    vmwait<21>();
    if (w == 4) fl = load_flags(rs, b, l);
    bar();
  }

  f32x4 a0 = {0, 0, 0, 0}, a1 = {0, 0, 0, 0}, a2 = {0, 0, 0, 0};
  v4u x1p;
  {
    const int pcp = 18 * g + 2 * w + (q >> 1), pcp9 = 18 * g + 16 + (q >> 1), prow = 16 * (q & 1) + tok;
    v4u ty[5], tx[5];
    auto off = [&](int i) INL { return ldws(rs, min(tid + 512 * i, 2303) * 16, O_YV + t32 * 36864); };
    v4u fr[3][2];
    {
      float sx = 0, sxx = 0;
#pragma unroll
      for (int it = 0; it < 5; it++) { tx[it] = *(LAS v4u*)(lds + L_XH + ((2 * rt + (tok >> 3)) * 9 + (min(kq + 4 * it, kq + 12 + (kq < 2 ? 4 : 0)) >> 1)) * 1024 + (tok & 7) * 128 + ((((kq & 1) * 4 + q) ^ (tok & 6)) << 4)); if (it < 4 || kq < 2) stat_acc(tx[it], sx, sxx); }
      const float t = stat_reduce(sx, sxx);
      if (l < 32) *(LAS float*)(lds + L_ST + (rt * 16 + tok) * 32 + kq * 8 + q * 4) = t;
    }
    retile_read(jm, lds, L_RT, w, l, fr);
    if (w == 4) {
      wait_flags(rs, fl, b, l, epoch, 2, ~0ull, alive);
      if (b == 0 && l == 0) __builtin_amdgcn_raw_buffer_store_b32(epoch, rs, 0, O_EW, 16);
    }
    bar();
    retile_emit(jm, lds, rs, w, l, fr);
    v4u wb[21];
    auto ldw2 = [&](int n) INL { const int i = n < 3 ? KS + n : n - 3; wb[i] = load_gw(rs, O_W2, g, w, l, i); };
#pragma unroll
    for (int n = 0; n < NA; n++) ldw2(n);
    __builtin_amdgcn_s_sleep(SLP2);
#pragma unroll
    for (int i = 0; i < 5; i++) ty[i] = off(i);
    SB();
    float xf[5][8]; unsigned qm[5][8];
    {
      f32x4 p0 = *(LAS f32x4*)(lds + L_ST + (rt * 16 + tok) * 32), p1 = *(LAS f32x4*)(lds + L_ST + (rt * 16 + tok) * 32 + 16);
      const float mean = ((p0.x + p0.z) + (p1.x + p1.z)) * (1.0f / D);
      const float r = __builtin_amdgcn_rsqf(fmaxf(((p0.y + p0.w) + (p1.y + p1.w)) * (1.0f / D) - mean * mean, 0.f) + EPS), mr = -mean * r;
#pragma unroll
      for (int it = 0; it < 5; it++) {
        const unsigned c[4] = {tx[it].x, tx[it].y, tx[it].z, tx[it].w};
#pragma unroll
        for (int e = 0; e < 4; e++) {
          xf[it][2 * e] = lo(c[e]); xf[it][2 * e + 1] = hi(c[e]);
          const unsigned qq = pk(__builtin_fmaf(xf[it][2 * e], r, mr), __builtin_fmaf(xf[it][2 * e + 1], r, mr));
          qm[it][2 * e] = qq & 0xffffu; qm[it][2 * e + 1] = qq & 0xffff0000u;
          pin(xf[it][2 * e]); pin(xf[it][2 * e + 1]); pin(qm[it][2 * e]); pin(qm[it][2 * e + 1]);
        }
      }
    }
    f32x4 cs = {0, 0, 0, 0}, c9 = {0, 0, 0, 0};
    {
      const v4u ones = {0x3f803f80u, 0x3f803f80u, 0x3f803f80u, 0x3f803f80u};
#pragma unroll
      for (int i = 0; i < KS; i++) cs = mfma16(wa[i], ones, cs);
      c9 = mfma16(wa[KS], ones, c9); c9 = mfma16(wa[KS + 1], ones, c9);
      if (w < 2) c9 = mfma16(wa[KS + 2], ones, c9);
      *(LAS f32x4*)(lds + L_C9 + w * 64 + q * 16) = c9;
      pin(cs);
    }
#pragma unroll
    for (int i = 0; i < 3; i++) { *(LAS v4u*)(lds + L_RT + ks9(w, i) * 2048 + l * 16) = wa[KS + i]; *(LAS v4u*)(lds + L_A9B + ks9(w, i) * 1024 + l * 16) = wb[KS + i]; }
    settle<5>(rs, ty, off, [&](int i, v4u v) INL { const int n = min(tid + 512 * i, 2303); *(LAS v4u*)(lds + L_Y + n * 16 + (n >= 1152 ? 32 : 0)) = v; }, alive);
    drain();
    bar();
    if (w == 7) publish_flag(rs, b, l, epoch + 3);
    const int a = (l & 15) >> 2, bb = l & 3;
    float sh = 0, shh = 0;
#pragma unroll
    for (int it = 0; it < 5; it++) if (it < 4 || kq < 2) {
      if (it < 4) {
#pragma unroll
        for (int n = NA + NB * it / 4; n < NA + NB * (it + 1) / 4; n++) ldw2(n);
      }
      const int ks = kq + 4 * it;
      LAS char* p = lds + L_Y + (16 * ks + 4 * q + (a >> 1)) * 64 + (a & 1) * (288 * 64 + 32) + rt * 32 + bb * 8;
      v2u y0 = trrd(p), y1 = trrd(p + 128);
      const unsigned yy[4] = {y0.x, y0.y, y1.x, y1.y};
      unsigned hp[4];
#pragma unroll
      for (int e = 0; e < 4; e++) hp[e] = pk(dot2(qm[it][2 * e], yy[e], xf[it][2 * e]), dot2(qm[it][2 * e + 1], yy[e], xf[it][2 * e + 1]));
      const v4u hv = {hp[0], hp[1], hp[2], hp[3]};
      *(LAS v4u*)(lds + L_XH + (rt * KS + ks) * 1024 + l * 16) = hv;
      stat_acc(hv, sh, shh);
    }
    { const float t = stat_reduce(sh, shh); if (l < 32) *(LAS float*)(lds + L_ST + 1024 + (rt * 16 + tok) * 32 + kq * 8 + q * 4) = t; }
    bar();
    f32x4 gc[2]; float r1[2], mr1[2];
    if (w < 2) {
      c9 = *(LAS f32x4*)(lds + L_C9 + q * 16);
#pragma unroll
      for (int i = 1; i < 8; i++) c9 += *(LAS f32x4*)(lds + L_C9 + i * 64 + q * 16);
    }
#pragma unroll
    for (int m = 0; m < 2; m++) {
      f32x4 p0 = *(LAS f32x4*)(lds + L_ST + 1024 + (m * 16 + tok) * 32), p1 = *(LAS f32x4*)(lds + L_ST + 1024 + (m * 16 + tok) * 32 + 16);
      gc[m] = p0 + p1;
    }
    small_gemm<2048>(wa, lds + L_XH, lds + L_RT, w, l, a0, a1, a2, [&](auto k_) INL {
      constexpr int ks = decltype(k_)::v;
      if constexpr (ks < 14) { sfor<NC * ks / 14, NC * (ks + 1) / 14>([&](auto n_) INL { ldw2(NA + NB + decltype(n_)::v); }); }
      if constexpr (ks == 4 || ks == 8) {
        constexpr int m = ks == 8;
        const float mean = (gc[m].x + gc[m].z) * (1.0f / D);
        const float var = fmaxf((gc[m].y + gc[m].w) * (1.0f / D) - mean * mean, 0.f);
        r1[m] = __builtin_amdgcn_rsqf(var + EPS); mr1[m] = -mean * r1[m];
        const float v2 = var * r1[m] * r1[m] + EPS, r2 = __builtin_amdgcn_rsqf(v2);
        gc[m] = gelu_consts(r1[m] * r2, (var + EPS) * r1[m] * v2 * r2);
        gc[m].w = -mean;
      }
    });
    const int dl = g == 0 ? 1 : g == 1 ? -1 : g == 2 ? 2 : -2;
    int tt = t0 + prow + dl;
    const bool wrap = tt < 0 || tt >= S;
    tt &= S - 1;
    auto gl = [&](f32x4 v, f32x4 c, f32x4 k) INL { v += c * k.w; return f32x4{gelu_ln(v.x, k), gelu_ln(v.y, k), gelu_ln(v.z, k), gelu_ln(v.w, k)}; };
    {
      v4u o = piece2(gl(a0, cs, gc[0]), gl(a1, cs, gc[1]));
      if (wrap) o = v4u{0, 0, 0, 0};
      stws(rs, o, frag_off(tt, pcp), O_HSH);
    }
    if (w < 2) {
      f32x4 t = gl(a2, c9, w ? gc[1] : gc[0]);
      v4u o = piece2(t, t);
      if (wrap) o = v4u{0, 0, 0, 0};
      if ((q & 1) == w) stws(rs, o, frag_off(tt, pcp9), O_HSH);
    }
    bar();
#pragma unroll
    for (int n = NA + NB + NC; n < 21; n++) ldw2(n);
    if (w == 7) fl = load_flags(rs, b, l);
    SB();
    auto xbinit = [&](int m, int col) INL {
      v2u hh = *(LAS v2u*)(lds + L_XH + frag_off(16 * m + tok, col >> 3) + (q & 1) * 8);
      const float r = m ? r1[1] : r1[0], mr = m ? mr1[1] : mr1[0];
      return f32x4{__builtin_fmaf(lo(hh.x), r, mr), __builtin_fmaf(hi(hh.x), r, mr), __builtin_fmaf(lo(hh.y), r, mr), __builtin_fmaf(hi(hh.y), r, mr)};
    };
    a0 = xbinit(0, 144 * g + 16 * w + 4 * q); a1 = xbinit(1, 144 * g + 16 * w + 4 * q);
    if (w < 2) a2 = xbinit(w, 144 * g + 128 + 4 * q);
    *(LAS v4u*)(lds + L_HS + w * 1024 + l * 16) = *(LAS v4u*)(lds + L_XH + frag_off(prow, pcp));
    if (w == 2) *(LAS v4u*)(lds + L_HS + 8192 + l * 16) = *(LAS v4u*)(lds + L_XH + frag_off(prow, pcp9));
    pin(a0); pin(a1); pin(a2);
    v4u tb[5];
    auto off2 = [&](int i) INL { return ldws(rs, min(tid + 512 * i, 2303) * 16, O_HSH + t32 * 36864); };
    SB();
    __builtin_amdgcn_s_sleep(SLP3);
#pragma unroll
    for (int i = 0; i < 5; i++) tb[i] = off2(i);
    settle<5>(rs, tb, off2, [&](int i, v4u v) INL { *(LAS v4u*)(lds + L_B2 + min(tid + 512 * i, 2303) * 16) = v; }, alive);
    if (w == 7) wait_flags(rs, fl, b, l, epoch, 3, ~0ull, alive);
    bar();
    v4u ring[RW];
    const int sbase = O_WM + ((b & 7) * 1296 + (w < 4 ? w * 180 : 144 + w * 144)) * 1024;
    small_gemm<1024>(wb, lds + L_B2, lds + L_A9B, w, l, a0, a1, a2, [&](auto k_) INL {
      constexpr int ks = decltype(k_)::v;
      if constexpr (ks < 14) { sfor<ND * ks / 14, ND * (ks + 1) / 14>([&](auto n_) INL { ring[decltype(n_)::v] = ring_load(rs, sbase, l * 16, decltype(n_)::v); }); }
    });
    x1p = piece2(a0, a1);
    stws(rs, x1p, frag_off(t0 + prow, pcp), O_X1);
    if (w < 2) { v4u o = piece2(a2, a2); if ((q & 1) == w) { stws(rs, o, frag_off(t0 + prow, pcp9), O_X1); *(LAS v4u*)(lds + L_HS + 9216 + l * 16) = o; } }
    bar();
#pragma unroll
    for (int i = ND; i < RW; i++) ring[i] = ring_load(rs, sbase, l * 16, i);
    __builtin_amdgcn_s_sleep(SLP4);
    v4u tq[9];
    const int xo = ((l >> 3) & 3) * 256 + (8 * (w & 1) + (l & 7)) * 16;
    auto off3 = [&](int i) INL { return ldws(rs, (l >> 5) * 1024 + xo, O_X1 + ((4 * tile64 + (w >> 1)) * KS + 2 * i) * 1024); };
#pragma unroll
    for (int i = 0; i < 9; i++) tq[i] = off3(i);
    SB();
    settle<9>(rs, tq, off3, [&](int i, v4u v) INL { *(LAS v4u*)(lds + L_X + (8 * i + (w >> 1)) * 1024 + (l >> 5) * 4096 + xo) = v; }, alive);
    bar();
    if (w >= 4) {
      mlp_body<4>(lds, rs, ring, tq, sbase, b, w, epoch, fl);
#pragma unroll
      for (int i = 0; i < RW; i++) asm volatile("" : "=v"(ring[i]));
#pragma unroll
      for (int i = 0; i < 9; i++) asm volatile("" : "=v"(tq[i]));
    }
    int wv = w;
    asm volatile("" : "+s"(wv));
    if (wv < 4) mlp_body<5>(lds, rs, ring, tq, sbase, b, w, epoch, fl);
  }
  {
    const int l = lane(), tok = l & 15, q = l >> 4;
    const int pcp = 18 * g + 2 * w + (q >> 1), pcp9 = 18 * g + 16 + (q >> 1), prow = 16 * (q & 1) + tok;
    unsigned sel_lo = 0x3f80u, sel_hi = 0x3f800000u;
    asm volatile("" : "+s"(sel_lo), "+s"(sel_hi));
    wait_flags(rs, fl, b, l, epoch, 4, 3ull << (t32 & ~1), alive);
    bar();
    __builtin_amdgcn_s_sleep(SLP5);
    auto finish = [&](v4u (&pp)[8], int cp, int hs, v4u xx) INL {
      const v4u hh = *(LAS v4u*)(lds + L_HS + hs * 1024 + l * 16);
      float s[8] = {lo(hh.x) + lo(xx.x), hi(hh.x) + hi(xx.x), lo(hh.y) + lo(xx.y), hi(hh.y) + hi(xx.y),
                    lo(hh.z) + lo(xx.z), hi(hh.z) + hi(xx.z), lo(hh.w) + lo(xx.w), hi(hh.w) + hi(xx.w)};
#pragma unroll
      for (int i = 0; i < 8; i++) pin(s[i]);
      const int po = frag_off(t0 + prow, cp);
      settle<8>(rs, pp, [&](int i) INL { return ldws(rs, po, O_PART + i * ACT); }, [&](int, v4u) INL {}, alive);
#pragma unroll
      for (int i = 0; i < 8; i++) {
        s[0] = dot2(pp[i].x, sel_lo, s[0]); s[1] = dot2(pp[i].x, sel_hi, s[1]); s[2] = dot2(pp[i].y, sel_lo, s[2]); s[3] = dot2(pp[i].y, sel_hi, s[3]);
        s[4] = dot2(pp[i].z, sel_lo, s[4]); s[5] = dot2(pp[i].z, sel_hi, s[5]); s[6] = dot2(pp[i].w, sel_lo, s[6]); s[7] = dot2(pp[i].w, sel_hi, s[7]);
      }
      *(v4u*)(out + (t0 + prow) * D + cp * 8) = v4u{pk(s[0], s[1]), pk(s[2], s[3]), pk(s[4], s[5]), pk(s[6], s[7])};
    };
    v4u pa[8], pc[8];
#pragma unroll
    for (int i = 0; i < 8; i++) pa[i] = ldws(rs, frag_off(t0 + prow, pcp), O_PART + i * ACT);
    if (w == 0) {
#pragma unroll
      for (int i = 0; i < 8; i++) pc[i] = ldws(rs, frag_off(t0 + prow, pcp9), O_PART + i * ACT);
    }
    SB();
    finish(pa, pcp, w, x1p);
    if (w == 0) finish(pc, pcp9, 8, *(LAS v4u*)(lds + L_HS + 9216 + l * 16));
  }
}

extern "C" void kernel_launch(void* const* d_in, const int* in_sizes, int n_in, void* d_out, int out_size, void* d_ws, size_t ws_size,
                              hipStream_t stream) {
  mega<<<256, 512, 0, stream>>>((const u16*)d_in[0], (const u16*)d_in[1], (const u16*)d_in[2], (const u16*)d_in[3], (const u16*)d_in[4],
                                (const u16*)d_in[5], (const u16*)d_in[7], (const u16*)d_in[8], (const u16*)d_in[9], (const u16*)d_in[10],
                                (char*)d_ws, (u16*)d_out);
}
```

```cpp
#include <hip/hip_runtime.h>
#include <stdint.h>
typedef unsigned short u16;
typedef unsigned v4u __attribute__((ext_vector_type(4)));
typedef unsigned v2u __attribute__((ext_vector_type(2)));
typedef float f32x2 __attribute__((ext_vector_type(2)));
typedef float f32x4 __attribute__((ext_vector_type(4)));
typedef float f32x16 __attribute__((ext_vector_type(16)));
typedef __bf16 bf16x8 __attribute__((ext_vector_type(8)));
typedef __bf16 bf16x2 __attribute__((ext_vector_type(2)));
typedef short s16x4 __attribute__((ext_vector_type(4)));
typedef __amdgpu_buffer_rsrc_t rsrc_t;
#define LAS __attribute__((address_space(3)))
#define DEV __device__ __forceinline__
#define SB() __builtin_amdgcn_sched_barrier(0)
#define INL __attribute__((always_inline))
template <int V> struct IC { static constexpr int v = V; };
template <int I, int N, class Fn> DEV void sfor(Fn&& f) { if constexpr (I < N) { f(IC<I>{}); sfor<I + 1, N>(f); } }

constexpr int S = 2048, D = 576, F = 4608;
constexpr int KS = 18;
constexpr int ACT = S * D * 2;
constexpr int WIN = D * D * 2;
constexpr int O_EW = 0, O_FLAG = 1024, O_UT = 16384, O_YV = O_UT + ACT, O_W1 = O_YV + ACT, O_W2 = O_W1 + WIN, O_HSH = O_YV + 3840 * 1024,
              O_X1 = O_HSH + ACT, O_PART = O_X1 + ACT, O_WM = O_PART + 8 * ACT, WS_USED = O_WM + 16 * WIN;
static_assert(O_W2 + WIN <= O_HSH, "ws map");
constexpr float EPS = 1e-5f;
constexpr unsigned SENT = 0xffffffffu, EPOCH_STEP = 0x9E3779B9u, NSTAGE = 3;
constexpr int SPIN_CAP = 1 << 14;
constexpr int NCONV = 216;
#define NPRE1 10
#define NPRE2 10

constexpr int L_X = 0, L_H = 73728, L_HS = 147456, L_ST = 157760, L_CS = L_ST + 2048, L_MISC = L_CS + 1024, L_BYTES = L_MISC + 64;
constexpr int L_XH = 0, L_RT = 36864;
constexpr int CU_SLOT = 95 * 80, CF_SLOT = 4192, CX_SLOT = 32 * 144;
constexpr int L_CU = L_H, L_CF = L_CU + 4 * CU_SLOT, L_CX = L_CF + 4 * CF_SLOT;
constexpr int L_Y = L_H, L_B2 = L_H, L_A9B = L_H + 36896;
static_assert(L_CX + 8 * CX_SLOT <= L_ST && L_BYTES <= 163840 && L_A9B <= L_CX && L_A9B + 18432 <= L_HS, "lds map");

DEV unsigned pk(float a, float b) { f32x2 v = {a, b}; return __builtin_bit_cast(unsigned, __builtin_convertvector(v, bf16x2)); }
DEV float lo(unsigned u) { return __builtin_bit_cast(float, u << 16); }
DEV float hi(unsigned u) { return __builtin_bit_cast(float, u & 0xffff0000u); }
DEV float asf(unsigned u) { return __builtin_bit_cast(float, u); }
DEV unsigned asu(float f) { return __builtin_bit_cast(unsigned, f); }
DEV float dot2(unsigned a, unsigned b, float c) {
  return __builtin_amdgcn_fdot2_f32_bf16(__builtin_bit_cast(bf16x2, a), __builtin_bit_cast(bf16x2, b), c, false);
}
DEV f32x4 mfma16(v4u a, v4u b, f32x4 c) {
  return __builtin_amdgcn_mfma_f32_16x16x32_bf16(__builtin_bit_cast(bf16x8, a), __builtin_bit_cast(bf16x8, b), c, 0, 0, 0);
}
DEV f32x16 mfma32(v4u a, v4u b, f32x16 c) {
  return __builtin_amdgcn_mfma_f32_32x32x16_bf16(__builtin_bit_cast(bf16x8, a), __builtin_bit_cast(bf16x8, b), c, 0, 0, 0);
}
DEV v2u trrd(LAS char* p) { return __builtin_bit_cast(v2u, __builtin_amdgcn_ds_read_tr16_b64_v4i16((LAS s16x4*)p)); }
DEV v4u ldws(rsrc_t rs, int voff, int soff) { return __builtin_amdgcn_raw_buffer_load_b128(rs, voff, soff, 16); }
DEV void stws(rsrc_t rs, v4u v, int voff, int soff) {
  __builtin_amdgcn_raw_buffer_store_b128(v, rs, voff, soff, 16);
  asm volatile("s_nop 1" :: "v"(v));
}
DEV void bar() { asm volatile("s_waitcnt lgkmcnt(0)\n\ts_barrier" ::: "memory"); }
DEV void drain() { asm volatile("s_waitcnt vmcnt(0)" ::: "memory"); }
DEV void swap16(unsigned& a, unsigned& b) { auto r = __builtin_amdgcn_permlane16_swap(a, b, false, false); unsigned x = r[0], y = r[1]; a = x; b = y; }
DEV void swap32(unsigned& a, unsigned& b) { auto r = __builtin_amdgcn_permlane32_swap(a, b, false, false); unsigned x = r[0], y = r[1]; a = x; b = y; }
DEV void glds16(const void* g, int ldsaddr) {
  unsigned keep;
  asm volatile("s_mov_b32 %0, m0\n\ts_mov_b32 m0, %2\n\ts_nop 0\n\tglobal_load_lds_dwordx4 %1, off\n\ts_mov_b32 m0, %0"
               : "=&s"(keep) : "v"(g), "s"(ldsaddr) : "memory");
}
DEV int lane() { int l; asm volatile("v_mbcnt_lo_u32_b32 %0, -1, 0\n\tv_mbcnt_hi_u32_b32 %0, -1, %0" : "=v"(l)); return l; }
template <class T> DEV void pin(T& v) { asm volatile("" : "+v"(v)); }
DEV unsigned mx4(unsigned m, v4u v) { m = max(max(m, v.x), v.y); return max(max(m, v.z), v.w); }
template <int N> DEV void vmwait() { asm volatile("s_waitcnt vmcnt(%0)" :: "n"(N) : "memory"); }
DEV float gelu_ln(float y, f32x4 c) {
  const float p = __builtin_fmaf(y * y, c.y, c.x);
  const float e = __builtin_amdgcn_exp2f(y * p);
  return y * __builtin_amdgcn_rcpf(__builtin_fmaf(e, c.z, c.z));
}
DEV f32x4 gelu_consts(float r, float rinv) {
  const float c0 = -2.0f * 0.7978845608028654f * 1.4426950408889634f, c1 = c0 * 0.044715f;
  return f32x4{r * c0, r * r * r * c1, rinv, 0.f};
}
DEV float stat_reduce(float s, float ss) {
  unsigned a = asu(s), b = asu(ss);
  swap16(a, b);
  float t = asf(a) + asf(b);
  unsigned c = asu(t), d = c;
  swap32(c, d);
  return asf(c) + asf(d);
}
DEV void stat_acc(v4u v, float& s, float& ss) {
  const unsigned one = 0x3f803f80u;
  s = dot2(v.x, one, s); ss = dot2(v.x, v.x, ss); s = dot2(v.y, one, s); ss = dot2(v.y, v.y, ss);
  s = dot2(v.z, one, s); ss = dot2(v.z, v.z, ss); s = dot2(v.w, one, s); ss = dot2(v.w, v.w, ss);
}
DEV int pairbase(int g) { return 144 * g + ((g & 1) << 4); }
DEV int singlebase(int g) { return 144 * g + ((g & 1) ? 0 : 128); }
DEV int frag_off(int row, int colpiece) { return (((row >> 4) * KS + (colpiece >> 2)) * 64 + (colpiece & 3) * 16 + (row & 15)) * 16; }

DEV v4u load_flags(rsrc_t rs, int b, int l) { return ldws(rs, l * 16, O_FLAG + (b & 7) * 1024); }
DEV void wait_flags(rsrc_t rs, v4u f, int b, int l, unsigned epoch, unsigned stage, uint64_t need, int& alive) {
  const unsigned base = epoch + stage, lim = NSTAGE - stage;
  auto bad = [&]() INL { return __builtin_amdgcn_ballot_w64(max(max(f.x - base, f.y - base), max(f.z - base, f.w - base)) > lim) & need; };
  uint64_t any = bad();
#pragma nounroll
  while (any != 0 && alive > 0) {
    asm volatile("s_sleep 1" ::: "memory");
    f = load_flags(rs, b, l);
    --alive;
    any = bad();
  }
  asm volatile("" ::: "memory");
}
DEV void publish_flag(rsrc_t rs, int b, int l, unsigned val) {
  if (l < 8) __builtin_amdgcn_raw_buffer_store_b32(val, rs, l * 1024 + b * 4, O_FLAG, 16);
}
template <int N, class OFF, class USE>
DEV void settle(rsrc_t rs, v4u (&d)[N], OFF off, USE use, int& alive) {
  unsigned m = 0;
#pragma unroll
  for (int i = 0; i < N; i++) { m = mx4(m, d[i]); use(i, d[i]); }
  uint64_t any = __builtin_amdgcn_ballot_w64(m == SENT);
#pragma nounroll
  while (any != 0 && alive > 0) {
    asm volatile("" ::: "memory");
    if (m == SENT) {
#pragma unroll
      for (int i = 0; i < N; i++) d[i] = off(i);
      m = 0;
#pragma unroll
      for (int i = 0; i < N; i++) { m = mx4(m, d[i]); use(i, d[i]); }
    }
    --alive;
    any = __builtin_amdgcn_ballot_w64(m == SENT);
  }
}

struct Job { int kind, j, s; };
DEV Job job_main(int n) { Job jb; jb.kind = 2 + (n >= 144); n -= (n >= 144) * 144; jb.j = n & 7; jb.s = n >> 3; return jb; }
DEV int slot_ks(int s) {
  if (s == 12) return 4;
  if (s == 13) return 13;
  const int g = (s < 14 ? s : s - 14) & 3, k0 = pairbase(g) >> 5;
  return k0 + (s < 4 ? 0 : s < 8 ? 2 : s < 12 ? 1 : 3);
}
struct WPtr { const u16 *w1, *w2, *w3, *w4; };
DEV void retile_load(Job jb, int stage, int r0, int rstep, int n, int l, const WPtr& wp) {
  const u16* src; int ld = D;
  if (jb.kind < 2) src = (jb.kind ? wp.w2 : wp.w1) + 32 * jb.s;
  else if (jb.kind == 2) { src = wp.w3 + D * jb.j + 32 * slot_ks(jb.s); ld = F; }
  else src = wp.w4 + D * jb.j * D + 32 * slot_ks(jb.s);
  src += (l >> 2) * ld + (l & 3) * 8;
#pragma unroll
  for (int it = 0; it < 9; it++) {
    const int r = r0 + rstep * it;
    if (it < n && r < KS) {
      const u16* p = src + 32 * (jb.kind == 3 ? slot_ks(r) : r) * ld;
      glds16(p, stage + r * 2048); glds16(p + 16 * ld, stage + r * 2048 + 1024);
    }
  }
}
DEV void retile_read(Job jb, LAS char* lds, int stage, int w, int l, v4u (&fr)[3][2]) {
  const int q = l >> 4, a = (l & 15) >> 2, bb = l & 3;
  const bool nat = jb.kind < 2 || jb.s == 12 || jb.s == 13;
  LAS char* p = lds + stage + w * 2048 + (8 * q + a) * 64 + (nat ? 8 * bb : 16 * bb);
  const int ts = nat ? 32 : 8;
#pragma unroll
  for (int it = 0; it < 3; it++)
#pragma unroll
    for (int t = 0; t < 2; t++) {
      if (it < 2 || w < 2) { v2u x0 = trrd(p + it * 16384 + t * ts), x1 = trrd(p + it * 16384 + t * ts + 256); fr[it][t] = v4u{x0.x, x0.y, x1.x, x1.y}; }
      else fr[it][t] = v4u{0, 0, 0, 0};
    }
  if (!(jb.kind & 1)) {
    const v4u ones = {0x3f803f80u, 0x3f803f80u, 0x3f803f80u, 0x3f803f80u};
#pragma unroll
    for (int t = 0; t < 2; t++) {
      f32x4 c = {0, 0, 0, 0};
#pragma unroll
      for (int it = 0; it < 3; it++) c = mfma16(ones, fr[it][t], c);
      *(LAS float*)(lds + L_CS + (t * 16 + (l & 15)) * 32 + w * 4) = c.x;
    }
  }
}
DEV void retile_emit(Job jb, LAS char* lds, rsrc_t rs, int w, int l, v4u (&fr)[3][2]) {
  if (!(jb.kind & 1)) {
#pragma unroll
    for (int t = 0; t < 2; t++) {
      const f32x4 p0 = *(LAS f32x4*)(lds + L_CS + (t * 16 + (l & 15)) * 32), p1 = *(LAS f32x4*)(lds + L_CS + (t * 16 + (l & 15)) * 32 + 16);
      const float mu = (((p0.x + p0.y) + (p0.z + p0.w)) + ((p1.x + p1.y) + (p1.z + p1.w))) * (1.0f / D);
#pragma unroll
      for (int it = 0; it < 3; it++) if (it < 2 || w < 2) {
        const unsigned c0 = fr[it][t].x, c1 = fr[it][t].y, c2 = fr[it][t].z, c3 = fr[it][t].w;
        fr[it][t] = v4u{pk(lo(c0) - mu, hi(c0) - mu), pk(lo(c1) - mu, hi(c1) - mu), pk(lo(c2) - mu, hi(c2) - mu), pk(lo(c3) - mu, hi(c3) - mu)};
      }
    }
  }
  const int s = jb.s;
#pragma unroll
  for (int t = 0; t < 2; t++) {
    const int ww = s < 8 ? s : s < 12 ? s - 8 : s < 14 ? 2 * (s - 12) + t : s - 10, ti = s < 8 ? t : (s == 12 || s == 13) ? 4 : 2 + t;
    const int nt = ww < 4 ? 5 : 4, bs = jb.j * 1296 + (ww < 4 ? ww * 180 : 144 + ww * 144);
#pragma unroll
    for (int it = 0; it < 3; it++) if (it < 2 || w < 2) {
      const int r = w + 8 * it;
      int o;
      if (jb.kind < 2) o = (jb.kind ? O_W2 : O_W1) + ((2 * s + t) * KS + r) * 1024;
      else {
        const int c0 = jb.kind == 2 ? 0 : 32 * nt, c1 = jb.kind == 2 ? 36 : 32 * nt + 8, rr = jb.kind == 2 ? r : r - 14;
        const int f = rr < 0 ? 18 * nt + r * nt + ti : ti < 2 ? c0 + rr * 2 + ti : c1 + rr * (nt - 2) + ti - 2;
        o = O_WM + (bs + f) * 1024;
      }
      stws(rs, fr[it][t], l * 16, o);
    }
  }
}

template <bool NINE, int A9S, class SIDE>
DEV void small_gemm_t(const v4u (&wa)[21], LAS char* p0, LAS char* p1, LAS char* a9, f32x4& a0, f32x4& a1, f32x4& a2, SIDE side) {
  v4u b0[3], b1[3], wn[3];
  auto rd = [&](int ks) INL {
    b0[ks % 3] = *(LAS v4u*)(p0 + ks * 1024); b1[ks % 3] = *(LAS v4u*)(p1 + ks * 1024);
    if (NINE) wn[ks % 3] = *(LAS v4u*)(a9 + ks * A9S);
  };
  rd(0); rd(1);
  SB();
  sfor<0, KS>([&](auto k_) INL {
    constexpr int ks = decltype(k_)::v;
    if constexpr (ks + 2 < KS) rd(ks + 2);
    a0 = mfma16(wa[ks], b0[ks % 3], a0); a1 = mfma16(wa[ks], b1[ks % 3], a1);
    if constexpr (NINE) a2 = mfma16(wn[ks % 3], b0[ks % 3], a2);
    side(k_);
    SB();
  });
}
template <int A9S, class SIDE>
DEV void small_gemm(const v4u (&wa)[21], LAS char* bimg, LAS char* a9, int w, int l, f32x4& a0, f32x4& a1, f32x4& a2, SIDE side) {
  LAS char* p = bimg + l * 16;
  if (w < 2) {
    f32x4 x = w ? a1 : a0, y = w ? a0 : a1;
    small_gemm_t<true, A9S>(wa, p + w * KS * 1024, p + (w ^ 1) * KS * 1024, a9 + l * 16, x, y, a2, side);
    a0 = w ? y : x; a1 = w ? x : y;
  } else small_gemm_t<false, A9S>(wa, p, p + KS * 1024, a9, a0, a1, a2, side);
}
DEV int ks9(int w, int i) { return i < 2 || w < 2 ? w + 8 * i : w + 8; }
DEV v4u load_gw(rsrc_t rs, int base, int g, int w, int l, int i) {
  return i < KS ? ldws(rs, l * 16 + (i & 3) * 1024, base + ((9 * g + w) * KS + (i & ~3)) * 1024)
                : ldws(rs, l * 16, base + ((9 * g + 8) * KS + ks9(w, i - KS)) * 1024);
}
DEV v4u piece2(f32x4 a, f32x4 b) {
  unsigned a0 = pk(a.x, a.y), a1 = pk(a.z, a.w), b0 = pk(b.x, b.y), b1 = pk(b.z, b.w);
  swap16(a0, b0); swap16(a1, b1);
  return v4u{a0, a1, b0, b1};
}

template <int NT> struct Seg {
  static constexpr int steps(int s) { return s < 2 ? 18 : s == 2 ? 8 : s == 3 ? 6 : 4; }
  static constexpr int nt(int s) { return s == 0 || s == 4 ? 2 : s == 1 || s == 5 ? NT - 2 : NT; }
  static constexpr int base(int s) { int b = 0; for (int i = 0; i < s; i++) b += steps(i) * nt(i); return b; }
  static constexpr int slot0(int s) { return s < 3 ? 0 : s == 3 ? 8 : 14; }
};
constexpr int RW = 16;
DEV v4u ring_load(rsrc_t rs, int sbase, int l16, int f) { return ldws(rs, l16 + (f & 3) * 1024, sbase + (f & ~3) * 1024); }

template <int NT>
DEV void mlp_body(LAS char* lds, rsrc_t rs, v4u (&ring)[RW], const v4u (&tx)[9], int sbase, int b, int w, unsigned epoch, v4u& fl
) {
  using G = Seg<NT>;
  constexpr int TOT = 36 * NT;
  const int g = w & 3, odd = g & 1, j = b & 7, tile = b >> 3;
  const int l16 = lane() * 16;
  LAS char* const bx = lds + L_X + l16;
  LAS char* const bh = lds + L_H + l16;
  auto bptr = [&](int s, int st) INL { return (s < 2 ? bx : bh) + (G::slot0(s) + st) * 4096; };
  f32x4 acc1[NT][4], acc2[NT][4];
#pragma unroll
  for (int i = 0; i < NT; i++)
#pragma unroll
    for (int m = 0; m < 4; m++) { acc1[i][m] = f32x4{0, 0, 0, 0}; acc2[i][m] = f32x4{0, 0, 0, 0}; }
  v4u bf[4];
#pragma unroll
  for (int m = 0; m < 4; m++) bf[m] = *(LAS v4u*)(bx + m * 1024);
  float t0 = 0, t1 = 0, sa = 0, sb = 0, qa = 0, qb = 0;
  f32x4 gc[2];
  unsigned go[4];
  LAS char *gcp = lds, *hp = lds, *hs = lds;
  const v4u sent = {SENT, SENT, SENT, SENT};
  auto gelu_chunk = [&](auto c_, auto tb_, auto per_) INL {
    constexpr int c = decltype(c_)::v, tb = decltype(tb_)::v, PER = decltype(per_)::v, P = c / 10, k = c % 10, m = P / PER, e = 2 * (P % PER);
    const f32x4 av = acc1[e < 8 ? tb + (e >> 2) : NT - 1][m], kc = gc[m & 1];
    const float y0 = av[e & 3], y1 = av[(e & 3) + 1];
    if constexpr (k == 0) { t0 = y0 * y0; pin(t0); t1 = y1 * y1; }
    if constexpr (k == 1) { t0 = __builtin_fmaf(t0, kc.y, kc.x); pin(t0); t1 = __builtin_fmaf(t1, kc.y, kc.x); }
    if constexpr (k == 2 || k == 8) { t0 *= y0; pin(t0); t1 *= y1; }
    if constexpr (k == 3) t0 = __builtin_amdgcn_exp2f(t0);
    if constexpr (k == 4) t1 = __builtin_amdgcn_exp2f(t1);
    if constexpr (k == 5) { t0 = __builtin_fmaf(t0, kc.z, kc.z); pin(t0); t1 = __builtin_fmaf(t1, kc.z, kc.z); }
    if constexpr (k == 6) t0 = __builtin_amdgcn_rcpf(t0);
    if constexpr (k == 7) t1 = __builtin_amdgcn_rcpf(t1);
    if constexpr (k == 9) {
      go[(e >> 1) & 3] = pk(t0, t1);
      if constexpr (e == 6) *(LAS v4u*)(hp + m * 1024) = v4u{go[0], go[1], go[2], go[3]};
      if constexpr (e == 10) *(LAS v2u*)(hs + m * 1024) = v2u{go[0], go[1]};
      if constexpr (e == 0 && m < 3) gc[(m + 1) & 1] = *(LAS f32x4*)(gcp + (m + 1) * 256);
    }
  };
  auto gelu_setup = [&](int slot, bool single) INL {
    gcp = lds + L_ST + (l16 & 255);
    gc[0] = *(LAS f32x4*)gcp;
    hp = bh + slot * 4096;
    if (single) hs = lds + L_H + (12 + (g >> 1)) * 4096 + (2 * odd + (l16 >> 9)) * 256 + (l16 & 255) + ((l16 >> 5) & 8);
  };
  auto stat_chunk = [&](auto c_) INL {
    constexpr int c = decltype(c_)::v, d = c / 3, k = c % 3;
    if constexpr (k == 0) { const unsigned v = tx[d >> 2][d & 3]; t0 = lo(v); t1 = hi(v); }
    if constexpr (k == 1) { sa += t0; pin(sa); sb += t1; }
    if constexpr (k == 2) { qa = __builtin_fmaf(t0, t0, qa); pin(qa); qb = __builtin_fmaf(t1, t1, qb); }
  };
  const int pdst = O_PART + j * ACT + 4 * tile * KS * 1024, ks0 = (pairbase(g) >> 5) + (NT == 5 ? 0 : 2);
  auto store_pair = [&](int pp, int m) INL {
    f32x4 a = acc2[2 * pp][m], c = acc2[2 * pp + 1][m];
    stws(rs, v4u{pk(a.x, a.y), pk(a.z, a.w), pk(c.x, c.y), pk(c.z, c.w)}, l16, pdst + (m * KS + ks0 + pp) * 1024);
  };
  sfor<0, 6>([&](auto s_) INL {
    constexpr int s = decltype(s_)::v, nst = G::steps(s), n = G::nt(s), tb = (s == 1 || s == 5) ? 2 : 0;
    constexpr int NCH = s == 0 ? 108 : s == 1 ? 160 : s == 2 && NT == 5 ? 240 : s == 3 && NT == 4 ? 160 : 0;
    constexpr int SPAN = s == 0 ? 116 : (nst - 1) * 4 * n - 2;
    sfor<0, nst>([&](auto st_c) INL {
      constexpr int st = decltype(st_c)::v;
      constexpr bool last = st == nst - 1;
      if constexpr (last && s == 1) vmwait<RW>();
      if constexpr (last && s < 4) bar();
      if constexpr (last && s == 1 && NT == 4) { if (w == 7) publish_flag(rs, b, l16 >> 4, epoch + 3); }
      if constexpr (last && s == 0) gelu_setup(w, false);
      if constexpr (last && s == (NT == 5 ? 1 : 2)) gelu_setup(NT == 5 ? 8 + g : 14 + g, NT == 5);
      constexpr int f0 = G::base(s) + st * n;
      LAS char* np = last ? (s < 5 ? bptr(s + 1, 0) : lds) : bptr(s, st + 1);
      sfor<0, 4 * n>([&](auto k_) INL {
        constexpr int k = decltype(k_)::v, m = k / n, i = k % n, u = 4 * n * st + k, f = f0 + i;
        if constexpr (s < 2) acc1[tb + i][m] = mfma16(ring[f % RW], bf[m], acc1[tb + i][m]);
        else acc2[tb + i][m] = mfma16(ring[f % RW], bf[m], acc2[tb + i][m]);
        if constexpr (i == n - 1 && !(last && s == 5)) bf[m] = *(LAS v4u*)(np + m * 1024);
        if constexpr (m == 3 && f + RW < TOT) ring[f % RW] = ring_load(rs, sbase, l16, f + RW);
        if constexpr (NCH > 0 && u < SPAN) sfor<u * NCH / SPAN, (u + 1) * NCH / SPAN>([&](auto c_) INL {
          if constexpr (s == 0) stat_chunk(c_);
          else gelu_chunk(c_, IC<(s == 1 ? 0 : 2)>{}, IC<(s == 2 ? 6 : 4)>{});
        });
        if constexpr (s == 0 && u == 118) {
          sa += sb; qa += qb;
          sa += asf(__builtin_amdgcn_update_dpp(0, asu(sa), 0x128, 0xf, 0xf, false));
          qa += asf(__builtin_amdgcn_update_dpp(0, asu(qa), 0x128, 0xf, 0xf, false));
        }
        if constexpr (s == 0 && u == 120) sa = stat_reduce(sa, qa);
        if constexpr (s == 0 && u == 123) {
          unsigned a = asu(sa), c = a;
          swap16(a, c);
          const float mean = asf(a) * (1.0f / D);
          qa = fmaxf(asf(c) * (1.0f / D) - mean * mean, 0.f) + EPS;
        }
        if constexpr (s == 0 && u == 126) { const float r = __builtin_amdgcn_rsqf(qa); *(LAS f32x4*)(lds + L_ST + w * 128 + (l16 & 127)) = gelu_consts(r, qa * r); }
        if constexpr (s < 2 && k == 1 && (18 * s + st) % 3 == 1 && 18 * s + st < 27) stws(rs, sent, l16, pdst + (w * 9 + (18 * s + st) / 3) * 1024);
        if constexpr (s == 4 && u == 0) fl = load_flags(rs, b, l16 >> 4);
        if constexpr (s == 5 && st == 0 && i == 0) store_pair(0, m);
        if constexpr (s == 5 && last && m > 0 && i == n - 1) store_pair(1, m - 1);
        SB();
      });
    });
  });
  store_pair(1, 3);
  if (NT == 5) {
    const int l = lane(), q = l >> 4, cp = (singlebase(g) >> 3) + (q >> 1);
#pragma unroll
    for (int m = 0; m < 4; m += 2) stws(rs, piece2(acc2[4][m], acc2[4][m + 1]), frag_off(16 * (m + (q & 1)) + (l & 15), cp), pdst);
  }
}

__global__ __launch_bounds__(512, 2) void mega(const u16* __restrict__ x, const u16* __restrict__ kin, const u16* __restrict__ vin,
                                               const u16* __restrict__ sk, const u16* __restrict__ filt, const u16* __restrict__ Dv,
                                               const u16* __restrict__ w1, const u16* __restrict__ w2, const u16* __restrict__ w3,
                                               const u16* __restrict__ w4, char* ws, u16* out) {
  __shared__ __attribute__((aligned(16))) char smem[L_BYTES];
  LAS char* lds = (LAS char*)smem;
  const int ldsb = (int)(uintptr_t)lds;
  const int tid = threadIdx.x, l = tid & 63, w = __builtin_amdgcn_readfirstlane(tid >> 6), b = blockIdx.x;
  const rsrc_t rs = __builtin_amdgcn_make_buffer_rsrc((void*)ws, 0, WS_USED, 0x00020000);
  const v4u sent = {SENT, SENT, SENT, SENT};
  const WPtr wp = {w1, w2, w3, w4};
  const bool conv = b < NCONV;
  const int tok = l & 15, q = l >> 4;
  int alive = SPIN_CAP;

  unsigned epoch = 0;
  const int pb = 8 * (b / 3) + 2 * (b % 3);
  auto chan = [&](int p) INL { return p < 288 ? 2 * p : 2 * (p - 288) + 1; };
  Job je; je.kind = (b - NCONV) & 1; je.j = 0; je.s = (b - NCONV) >> 1;
  const bool early = !conv && b < NCONV + 36;
  {
    unsigned kk[11], vv[8], dv = 0; v4u sw;
    const int cp = min(tid, 287);
    if (w < 5) {
      const unsigned* kp = (const unsigned*)(kin + (8 * b - 3) * D) + cp;
      const unsigned* vp = (const unsigned*)(vin + 8 * b * D) + cp;
#pragma unroll
      for (int i = 0; i < 11; i++) kk[i] = (i >= 3 || b > 0) ? kp[i * (D / 2)] : 0u;
#pragma unroll
      for (int i = 0; i < 8; i++) vv[i] = vp[i * (D / 2)];
      sw = *(const v4u*)(sk + cp * 8);
      if (conv && w == 4) dv = Dv[chan(pb + (l & 3))];
    } else {
      if (w == 7) epoch = __builtin_amdgcn_raw_buffer_load_b32(rs, 0, O_EW, 16) + EPOCH_STEP;
#pragma unroll
      for (int i = 0; i < 8; i++) { const int n = (w - 5) * 8 + i; stws(rs, sent, l * 16, n < 9 ? O_UT + b * 9216 + n * 1024 : O_YV + b * 15360 + (n - 9) * 1024); }
      if (w < 7 && conv) {
#pragma unroll
        for (int i = 0; i < 8; i++) { const int sl = 2 * (w - 5) + (i >> 2); glds16(filt + chan(pb + sl) * S + ((i & 3) * 64 + l) * 8, ldsb + L_CF + sl * CF_SLOT + 80 + (i & 3) * 1024); }
        vmwait<8>();
      } else if (w < 7 && early) { retile_load(je, ldsb + L_RT, 9 * (w - 5), 1, 9, l, wp); vmwait<18>(); }
      else drain();
    }
    if (conv) {
      for (int i = tid; i < 4 * 155; i += 512) *(LAS v4u*)(lds + L_CU + (i / 155) * CU_SLOT + (i % 155) * 16) = v4u{0, 0, 0, 0};
      if (tid < 24) { const int sl = tid / 6, pc = tid % 6; *(LAS v4u*)(lds + L_CF + sl * CF_SLOT + (pc < 5 ? pc * 16 : 80 + 4096)) = v4u{0, 0, 0, 0}; }
    }
    bar();
    if (w == 7) { publish_flag(rs, b, l, epoch + 1); *(LAS unsigned*)(lds + L_MISC + 16) = epoch; }
    if (w < 5) {
      float u0[8], u1[8];
      unsigned ql[11], qh[11];
#pragma unroll
      for (int i = 1; i < 11; i++) { ql[i] = __builtin_amdgcn_perm(kk[i - 1], kk[i], 0x05040100u); qh[i] = __builtin_amdgcn_perm(kk[i - 1], kk[i], 0x07060302u); }
#pragma unroll
      for (int r = 0; r < 8; r++) {
        u0[r] = dot2(ql[r + 3], sw.x, dot2(ql[r + 1], sw.y, 0.f)) * lo(vv[r]);
        u1[r] = dot2(qh[r + 3], sw.z, dot2(qh[r + 1], sw.w, 0.f)) * hi(vv[r]);
      }
      if (tid < 288) {
        stws(rs, v4u{pk(u0[7], u0[6]), pk(u0[5], u0[4]), pk(u0[3], u0[2]), pk(u0[1], u0[0])}, cp * 16, O_UT + b * 9216);
        stws(rs, v4u{pk(u1[7], u1[6]), pk(u1[5], u1[4]), pk(u1[3], u1[2]), pk(u1[1], u1[0])}, cp * 16, O_UT + b * 9216 + 4608);
      }
      if (w == 4) *(LAS unsigned*)(lds + L_MISC + (l & 3) * 4) = dv << 16;
    }
    if (w == 5 || w == 6) { if (early) drain(); }
    if (w == 7) {
      wait_flags(rs, load_flags(rs, b, l), b, l, epoch, 1, ~0ull, alive);
      if (b == 0 && l == 0) __builtin_amdgcn_raw_buffer_store_b32(epoch, rs, 0, O_EW, 16);
    }
    bar();
  }

  const int tile64 = b >> 3, g = b & 3, t32 = b >> 2, t0 = 32 * t32;
  const int rt = w & 1, kq = w >> 1;
  v4u wa[21];
  const Job jm = job_main(b);
  auto stage_late = [&]() INL {
    retile_load(jm, ldsb + L_RT, w, 8, 3, l, wp);
#pragma unroll
    for (int it = 0; it < 5; it++) if (it < 4 || kq < 2)
      glds16(x + (t0 + 16 * rt + (l >> 2)) * D + 32 * (kq + 4 * it) + 8 * (l & 3), ldsb + L_XH + (rt * KS + kq + 4 * it) * 1024);
  };
  if (conv) {
    const int part = b % 3, u24 = 8 * part + w, c8 = u24 / 3, r3 = u24 % 3;
    const int type = c8 == 2 ? (r3 + 1) % 3 : r3;
    const int slot = c8 - 2 * part, nt = type != 0, cb = type == 2 ? 32 : 0;
    const int lam = l & 31, h = l >> 5;
    v4u tu[2];
    auto off = [&](int i) INL { return ldws(rs, ((tid >> 2) * D + (tid & 3)) * 16, O_UT + (i * 128 * D + pb) * 16); };
    tu[0] = off(0); tu[1] = off(1);
    settle<2>(rs, tu, off, [&](int i, v4u v) INL { *(LAS v4u*)(lds + L_CU + (tid & 3) * CU_SLOT + (31 + 32 * i + (tid >> 4)) * 80 + ((tid >> 2) & 3) * 16) = v; }, alive);
    bar();
    const int e0 = 32 * (cb - 1 + 16 * h) + lam + 33;
    LAS unsigned* fp = (LAS unsigned*)(lds + L_CF + slot * CF_SLOT) + (e0 >> 1);
    const unsigned sh = (e0 & 1) * 16;
    LAS char* ub = lds + L_CU + slot * CU_SLOT + (31 + 32 * nt + lam - cb - 16 * h - 15) * 80;
    unsigned dd[2][5];
    v4u R[2], bq[2][4];
    auto rdf = [&](int m) INL {
#pragma unroll
      for (int i = 0; i < 5; i++) dd[m & 1][i] = fp[16 * m + i];
    };
    auto mkr = [&](int m) INL {
      const unsigned* d = dd[m & 1];
      R[m & 1] = v4u{__builtin_amdgcn_alignbit(d[1], d[0], sh), __builtin_amdgcn_alignbit(d[2], d[1], sh), __builtin_amdgcn_alignbit(d[3], d[2], sh), __builtin_amdgcn_alignbit(d[4], d[3], sh)};
    };
    auto rdb = [&](int m) INL {
#pragma unroll
      for (int s = 0; s < 4; s++) bq[m & 1][s] = *(LAS v4u*)(ub + (15 - m) * 80 + 16 * s);
    };
    rdf(0); rdf(1); rdb(0);
    v2u uu[4];
#pragma unroll
    for (int o = 0; o < 4; o++) uu[o] = *(LAS v2u*)(lds + L_CU + slot * CU_SLOT + (31 + 32 * nt + lam) * 80 + 16 * o + (h ? 0 : 8));
    const float dcoef = type == 2 ? 0.f : *(LAS float*)(lds + L_MISC + slot * 4);
    if (b < 192) {
#pragma unroll
      for (int i = 0; i < 3; i++) stws(rs, sent, l * 16 + i * 1024, O_HSH + (b * 8 + w) * 3072);
    }
    f32x16 acc[4];
#pragma unroll
    for (int s = 1; s < 4; s++)
#pragma unroll
      for (int i = 0; i < 16; i++) acc[s][i] = 0.f;
#pragma unroll
    for (int o = 0; o < 4; o++) { acc[0][4 * o] = hi(uu[o].y) * dcoef; acc[0][4 * o + 1] = lo(uu[o].y) * dcoef; acc[0][4 * o + 2] = hi(uu[o].x) * dcoef; acc[0][4 * o + 3] = lo(uu[o].x) * dcoef; }
    mkr(0);
    SB();
    sfor<0, 16>([&](auto m_) INL {
      constexpr int m = decltype(m_)::v;
      mkr(m + 1);
      if constexpr (m < 15) { rdf(m + 2); rdb(m + 1); }
      acc[0] = mfma32(R[(m + 1) & 1], bq[m & 1][0], acc[0]);
#pragma unroll
      for (int s = 1; s < 4; s++) {
        const bool c = lam < 32 - 8 * s;
        const v4u A = R[(m + 1) & 1], Z = R[m & 1];
        acc[s] = mfma32(v4u{c ? A.x : Z.x, c ? A.y : Z.y, c ? A.z : Z.z, c ? A.w : Z.w}, bq[m & 1][s], acc[s]);
      }
      SB();
    });
#pragma unroll
    for (int o = 0; o < 4; o++) {
      f32x4 y;
#pragma unroll
      for (int e = 0; e < 4; e++) { const int i = 4 * o + e; y[e] = (acc[0][i] + acc[1][(i + 12) & 15]) + (acc[2][(i + 8) & 15] + acc[3][(i + 4) & 15]); }
      *(LAS f32x4*)(lds + L_CX + w * CX_SLOT + lam * 144 + 32 * o + 16 * h) = y;
    }
    vmwait<0>();
    bar();
    LAS char* cx = lds + L_CX + (l >> 2) * 144 + (l & 3) * 32;
    const int yo = (l >> 2) * D * 64 + (l & 3) * 16, ys = O_YV + (32 * nt * D + pb + slot) * 64;
    if (type == 0) {
#pragma unroll
      for (int i = 0; i < 2; i++) {
        const f32x4 a = *(LAS f32x4*)(cx + w * CX_SLOT + i * 2304), c = *(LAS f32x4*)(cx + w * CX_SLOT + i * 2304 + 16);
        stws(rs, v4u{pk(a.x, a.y), pk(a.z, a.w), pk(c.x, c.y), pk(c.z, c.w)}, yo, ys + i * 16 * D * 64);
      }
    } else {
      LAS char* p = cx + (w + 1 - type) * CX_SLOT + (type - 1) * 2304;
      const f32x4 a = *(LAS f32x4*)p + *(LAS f32x4*)(p + CX_SLOT), c = *(LAS f32x4*)(p + 16) + *(LAS f32x4*)(p + CX_SLOT + 16);
      stws(rs, v4u{pk(a.x, a.y), pk(a.z, a.w), pk(c.x, c.y), pk(c.z, c.w)}, yo, ys + (type - 1) * 16 * D * 64);
    }
    stage_late();
#pragma unroll
    for (int i = 0; i < 21; i++) wa[i] = load_gw(rs, O_W1, g, w, l, i);
  } else {
    v4u fr[3][2];
    const Job jx = job_main(b + 40);
    if (b < NCONV + 32) retile_load(jx, ldsb + L_H, w, 8, 3, l, wp);
    if (early) {
      retile_read(je, lds, L_RT, w, l, fr);
      bar();
      retile_emit(je, lds, rs, w, l, fr);
    }
    stage_late();
    if (b < NCONV + 32) {
      vmwait<8>();
      retile_read(jx, lds, L_H, w, l, fr);
      retile_emit(jx, lds, rs, w, l, fr);
    }
#pragma unroll
    for (int i = 0; i < 21; i++) wa[i] = load_gw(rs, O_W1, g, w, l, i);
    bar();
  }

  f32x4 a0 = {0, 0, 0, 0}, a1 = {0, 0, 0, 0}, a2 = {0, 0, 0, 0};
  v4u fl = sent;
  {
    const int pcp = 18 * g + 2 * w + (q >> 1), pcp9 = 18 * g + 16 + (q >> 1), prow = 16 * (q & 1) + tok;
    v4u ty[5], tx[5];
    auto off = [&](int i) INL { return ldws(rs, min(tid + 512 * i, 2303) * 16, O_YV + t32 * 36864); };
#pragma unroll
    for (int i = 0; i < 5; i++) ty[i] = off(i);
    vmwait<26>();
    v4u fr[3][2];
    {
      float sx = 0, sxx = 0;
#pragma unroll
      for (int it = 0; it < 5; it++) { tx[it] = *(LAS v4u*)(lds + L_XH + (rt * KS + min(kq + 4 * it, kq + 12 + (kq < 2 ? 4 : 0))) * 1024 + (tok * 4 + q) * 16); if (it < 4 || kq < 2) stat_acc(tx[it], sx, sxx); }
      const float t = stat_reduce(sx, sxx);
      if (l < 32) *(LAS float*)(lds + L_ST + (rt * 16 + tok) * 32 + kq * 8 + q * 4) = t;
    }
    retile_read(jm, lds, L_RT, w, l, fr);
    bar();
    retile_emit(jm, lds, rs, w, l, fr);
    float xf[5][8]; unsigned qm[5][8];
    {
      f32x4 p0 = *(LAS f32x4*)(lds + L_ST + (rt * 16 + tok) * 32), p1 = *(LAS f32x4*)(lds + L_ST + (rt * 16 + tok) * 32 + 16);
      const float mean = ((p0.x + p0.z) + (p1.x + p1.z)) * (1.0f / D);
      const float r = __builtin_amdgcn_rsqf(fmaxf(((p0.y + p0.w) + (p1.y + p1.w)) * (1.0f / D) - mean * mean, 0.f) + EPS), mr = -mean * r;
#pragma unroll
      for (int it = 0; it < 5; it++) {
        const unsigned c[4] = {tx[it].x, tx[it].y, tx[it].z, tx[it].w};
#pragma unroll
        for (int e = 0; e < 4; e++) {
          xf[it][2 * e] = lo(c[e]); xf[it][2 * e + 1] = hi(c[e]);
          const unsigned qq = pk(__builtin_fmaf(xf[it][2 * e], r, mr), __builtin_fmaf(xf[it][2 * e + 1], r, mr));
          qm[it][2 * e] = qq & 0xffffu; qm[it][2 * e + 1] = qq & 0xffff0000u;
          pin(xf[it][2 * e]); pin(xf[it][2 * e + 1]); pin(qm[it][2 * e]); pin(qm[it][2 * e + 1]);
        }
      }
    }
    settle<21>(rs, wa, [&](int i) INL { return load_gw(rs, O_W1, g, w, l, i); },
               [&](int i, v4u v) INL { if (i >= KS) *(LAS v4u*)(lds + L_RT + ks9(w, i - KS) * 2048 + l * 16) = v; }, alive);
    settle<5>(rs, ty, off, [&](int i, v4u v) INL { const int n = min(tid + 512 * i, 2303); *(LAS v4u*)(lds + L_Y + n * 16 + (n >= 1152 ? 32 : 0)) = v; }, alive);
    bar();
    const int a = (l & 15) >> 2, bb = l & 3;
    float sh = 0, shh = 0;
#pragma unroll
    for (int it = 0; it < 5; it++) if (it < 4 || kq < 2) {
      const int ks = kq + 4 * it;
      LAS char* p = lds + L_Y + (16 * ks + 4 * q + (a >> 1)) * 64 + (a & 1) * (288 * 64 + 32) + rt * 32 + bb * 8;
      v2u y0 = trrd(p), y1 = trrd(p + 128);
      const unsigned yy[4] = {y0.x, y0.y, y1.x, y1.y};
      unsigned hp[4];
#pragma unroll
      for (int e = 0; e < 4; e++) hp[e] = pk(dot2(qm[it][2 * e], yy[e], xf[it][2 * e]), dot2(qm[it][2 * e + 1], yy[e], xf[it][2 * e + 1]));
      const v4u hv = {hp[0], hp[1], hp[2], hp[3]};
      *(LAS v4u*)(lds + L_XH + (rt * KS + ks) * 1024 + l * 16) = hv;
      stat_acc(hv, sh, shh);
    }
    { const float t = stat_reduce(sh, shh); if (l < 32) *(LAS float*)(lds + L_ST + 1024 + (rt * 16 + tok) * 32 + kq * 8 + q * 4) = t; }
    drain();
    bar();
    if (w == 7) publish_flag(rs, b, l, epoch + 2);
    f32x4 gc[2]; float r1[2], mr1[2];
#pragma unroll
    for (int m = 0; m < 2; m++) {
      f32x4 p0 = *(LAS f32x4*)(lds + L_ST + 1024 + (m * 16 + tok) * 32), p1 = *(LAS f32x4*)(lds + L_ST + 1024 + (m * 16 + tok) * 32 + 16);
      gc[m] = p0 + p1;
    }
    v4u wb[21];
    small_gemm<2048>(wa, lds + L_XH, lds + L_RT, w, l, a0, a1, a2, [&](auto k_) INL {
      constexpr int ks = decltype(k_)::v;
      if constexpr (ks < NPRE1) wb[ks] = load_gw(rs, O_W2, g, w, l, ks);
      if constexpr (ks == 4 || ks == 8) {
        constexpr int m = ks == 8;
        const float mean = (gc[m].x + gc[m].z) * (1.0f / D);
        const float var = fmaxf((gc[m].y + gc[m].w) * (1.0f / D) - mean * mean, 0.f);
        r1[m] = __builtin_amdgcn_rsqf(var + EPS); mr1[m] = -mean * r1[m];
        const float v2 = var * r1[m] * r1[m] + EPS, r2 = __builtin_amdgcn_rsqf(v2);
        gc[m] = gelu_consts(r1[m] * r2, (var + EPS) * r1[m] * v2 * r2);
      }
    });
    const int dl = g == 0 ? 1 : g == 1 ? -1 : g == 2 ? 2 : -2;
    int tt = t0 + prow + dl;
    const bool wrap = tt < 0 || tt >= S;
    tt &= S - 1;
    auto gl = [&](f32x4 v, f32x4 k) INL { return f32x4{gelu_ln(v.x, k), gelu_ln(v.y, k), gelu_ln(v.z, k), gelu_ln(v.w, k)}; };
    {
      v4u o = piece2(gl(a0, gc[0]), gl(a1, gc[1]));
      if (wrap) o = v4u{0, 0, 0, 0};
      stws(rs, o, frag_off(tt, pcp), O_HSH);
    }
    if (w < 2) {
      f32x4 t = gl(a2, w ? gc[1] : gc[0]);
      v4u o = piece2(t, t);
      if (wrap) o = v4u{0, 0, 0, 0};
      if ((q & 1) == w) stws(rs, o, frag_off(tt, pcp9), O_HSH);
    }
    SB();
#pragma unroll
    for (int i = NPRE1; i < 21; i++) wb[i] = load_gw(rs, O_W2, g, w, l, i);
    v4u tb[5];
    auto off2 = [&](int i) INL { return ldws(rs, min(tid + 512 * i, 2303) * 16, O_HSH + t32 * 36864); };
#pragma unroll
    for (int i = 0; i < 5; i++) tb[i] = off2(i);
    if (w == 7) fl = load_flags(rs, b, l);
    SB();
    auto xbinit = [&](int m, int col) INL {
      v2u hh = *(LAS v2u*)(lds + L_XH + frag_off(16 * m + tok, col >> 3) + (q & 1) * 8);
      const float r = m ? r1[1] : r1[0], mr = m ? mr1[1] : mr1[0];
      return f32x4{__builtin_fmaf(lo(hh.x), r, mr), __builtin_fmaf(hi(hh.x), r, mr), __builtin_fmaf(lo(hh.y), r, mr), __builtin_fmaf(hi(hh.y), r, mr)};
    };
    a0 = xbinit(0, 144 * g + 16 * w + 4 * q); a1 = xbinit(1, 144 * g + 16 * w + 4 * q);
    if (w < 2) a2 = xbinit(w, 144 * g + 128 + 4 * q);
    *(LAS v4u*)(lds + L_HS + w * 1024 + l * 16) = *(LAS v4u*)(lds + L_XH + frag_off(prow, pcp));
    if (w == 2) *(LAS v4u*)(lds + L_HS + 8192 + l * 16) = *(LAS v4u*)(lds + L_XH + frag_off(prow, pcp9));
    pin(a0); pin(a1); pin(a2);
    settle<21>(rs, wb, [&](int i) INL { return load_gw(rs, O_W2, g, w, l, i); },
               [&](int i, v4u v) INL { if (i >= KS) *(LAS v4u*)(lds + L_A9B + ks9(w, i - KS) * 1024 + l * 16) = v; }, alive);
    settle<5>(rs, tb, off2, [&](int i, v4u v) INL { *(LAS v4u*)(lds + L_B2 + min(tid + 512 * i, 2303) * 16) = v; }, alive);
    if (w == 7) wait_flags(rs, fl, b, l, epoch, 2, ~0ull, alive);
    bar();
    v4u ring[RW];
    const int sbase = O_WM + ((b & 7) * 1296 + (w < 4 ? w * 180 : 144 + w * 144)) * 1024;
    small_gemm<1024>(wb, lds + L_B2, lds + L_A9B, w, l, a0, a1, a2, [&](auto k_) INL {
      constexpr int ks = decltype(k_)::v;
      if constexpr (ks < NPRE2) ring[ks] = ring_load(rs, sbase, l * 16, ks);
    });
    stws(rs, piece2(a0, a1), frag_off(t0 + prow, pcp), O_X1);
    if (w < 2) { v4u o = piece2(a2, a2); if ((q & 1) == w) stws(rs, o, frag_off(t0 + prow, pcp9), O_X1); }
    SB();
#pragma unroll
    for (int i = NPRE2; i < RW; i++) ring[i] = ring_load(rs, sbase, l * 16, i);
    v4u tq[9];
    const int xo = ((l >> 3) & 3) * 256 + (8 * (w & 1) + (l & 7)) * 16;
    auto off3 = [&](int i) INL { return ldws(rs, (l >> 5) * 1024 + xo, O_X1 + ((4 * tile64 + (w >> 1)) * KS + 2 * i) * 1024); };
#pragma unroll
    for (int i = 0; i < 9; i++) tq[i] = off3(i);
    SB();
    epoch = __builtin_amdgcn_readfirstlane(*(LAS unsigned*)(lds + L_MISC + 16));
    settle<9>(rs, tq, off3, [&](int i, v4u v) INL { *(LAS v4u*)(lds + L_X + (8 * i + (w >> 1)) * 1024 + (l >> 5) * 4096 + xo) = v; }, alive);
    bar();
    if (w >= 4) {
      mlp_body<4>(lds, rs, ring, tq, sbase, b, w, epoch, fl);
#pragma unroll
      for (int i = 0; i < RW; i++) asm volatile("" : "=v"(ring[i]));
#pragma unroll
      for (int i = 0; i < 9; i++) asm volatile("" : "=v"(tq[i]));
    }
    int wv = w;
    asm volatile("" : "+s"(wv));
    if (wv < 4) mlp_body<5>(lds, rs, ring, tq, sbase, b, w, epoch, fl);
  }
  {
    const int l = lane(), tok = l & 15, q = l >> 4;
    const int pcp = 18 * g + 2 * w + (q >> 1), pcp9 = 18 * g + 16 + (q >> 1), prow = 16 * (q & 1) + tok;
    unsigned sel_lo = 0x3f80u, sel_hi = 0x3f800000u;
    asm volatile("" : "+s"(sel_lo), "+s"(sel_hi));
    wait_flags(rs, fl, b, l, epoch, 3, 3ull << (2 * tile64), alive);
    auto finish = [&](v4u (&pp)[8], int cp, int hs) INL {
      const v4u hh = *(LAS v4u*)(lds + L_HS + hs * 1024 + l * 16);
      const v4u xx = *(LAS v4u*)(lds + L_X + ((cp >> 2) * 4 + 2 * (t32 & 1) + (q & 1)) * 1024 + ((cp & 3) * 16 + tok) * 16);
      float s[8] = {lo(hh.x) + lo(xx.x), hi(hh.x) + hi(xx.x), lo(hh.y) + lo(xx.y), hi(hh.y) + hi(xx.y),
                    lo(hh.z) + lo(xx.z), hi(hh.z) + hi(xx.z), lo(hh.w) + lo(xx.w), hi(hh.w) + hi(xx.w)};
#pragma unroll
      for (int i = 0; i < 8; i++) pin(s[i]);
      const int po = frag_off(t0 + prow, cp);
      settle<8>(rs, pp, [&](int i) INL { return ldws(rs, po, O_PART + i * ACT); }, [&](int, v4u) INL {}, alive);
#pragma unroll
      for (int i = 0; i < 8; i++) {
        s[0] = dot2(pp[i].x, sel_lo, s[0]); s[1] = dot2(pp[i].x, sel_hi, s[1]); s[2] = dot2(pp[i].y, sel_lo, s[2]); s[3] = dot2(pp[i].y, sel_hi, s[3]);
        s[4] = dot2(pp[i].z, sel_lo, s[4]); s[5] = dot2(pp[i].z, sel_hi, s[5]); s[6] = dot2(pp[i].w, sel_lo, s[6]); s[7] = dot2(pp[i].w, sel_hi, s[7]);
      }
      *(v4u*)(out + (t0 + prow) * D + cp * 8) = v4u{pk(s[0], s[1]), pk(s[2], s[3]), pk(s[4], s[5]), pk(s[6], s[7])};
    };
    v4u pa[8], pc[8];
#pragma unroll
    for (int i = 0; i < 8; i++) pa[i] = ldws(rs, frag_off(t0 + prow, pcp), O_PART + i * ACT);
    if (w == 0) {
#pragma unroll
      for (int i = 0; i < 8; i++) pc[i] = ldws(rs, frag_off(t0 + prow, pcp9), O_PART + i * ACT);
    }
    SB();
    finish(pa, pcp, w);
    if (w == 0) finish(pc, pcp9, 8);
  }
}

extern "C" void kernel_launch(void* const* d_in, const int* in_sizes, int n_in, void* d_out, int out_size, void* d_ws, size_t ws_size,
                              hipStream_t stream) {
  mega<<<256, 512, 0, stream>>>((const u16*)d_in[0], (const u16*)d_in[1], (const u16*)d_in[2], (const u16*)d_in[3], (const u16*)d_in[4],
                                (const u16*)d_in[5], (const u16*)d_in[7], (const u16*)d_in[8], (const u16*)d_in[9], (const u16*)d_in[10],
                                (char*)d_ws, (u16*)d_out);
}
```

```cpp
#include <hip/hip_runtime.h>
#include <stdint.h>
typedef unsigned short u16;
typedef unsigned v4u __attribute__((ext_vector_type(4)));
typedef unsigned v2u __attribute__((ext_vector_type(2)));
typedef float f32x2 __attribute__((ext_vector_type(2)));
typedef float f32x4 __attribute__((ext_vector_type(4)));
typedef float f32x16 __attribute__((ext_vector_type(16)));
typedef __bf16 bf16x8 __attribute__((ext_vector_type(8)));
typedef __bf16 bf16x2 __attribute__((ext_vector_type(2)));
typedef short s16x4 __attribute__((ext_vector_type(4)));
typedef __amdgpu_buffer_rsrc_t rsrc_t;
#define LAS __attribute__((address_space(3)))
#define DEV __device__ __forceinline__
#define SB() __builtin_amdgcn_sched_barrier(0)
#define INL __attribute__((always_inline))
template <int V> struct IC { static constexpr int v = V; };
template <int I, int N, class Fn> DEV void sfor(Fn&& f) { if constexpr (I < N) { f(IC<I>{}); sfor<I + 1, N>(f); } }

constexpr int S = 2048, D = 576, F = 4608;
constexpr int KS = 18;
constexpr int ACT = S * D * 2;
constexpr int WIN = D * D * 2;
constexpr int O_EW = 0, O_FLAG = 1024, O_M2 = O_FLAG + 4 * 8192, O_UT = 40960, O_W1 = O_UT + 2 * ACT, O_W2 = O_W1 + WIN, O_YV = O_W2 + WIN, O_HSH = O_YV + ACT,
              O_X1 = O_HSH + ACT, O_PART = O_X1 + ACT, O_WM = O_PART + 8 * ACT, WS_USED = O_WM + 16 * WIN;
constexpr float EPS = 1e-5f;
constexpr unsigned SENT = 0xffffffffu, EPOCH_STEP = 0x9E3779B9u;
constexpr int SPIN_CAP = 1 << 14, OOB = 0x40000000;
constexpr int NCONV = 216;
#define NA 0
#define NB 5
#define NC 9
#define ND 9
#define W1S 10
#define WFL 3
#define FC 9
#define FL 6
#define FW 1
#define JX 2
#define RTM 2
#define XW4 12
#define T_FILT 0
#define T_CY 1
#define T_CH 2
#define T_XS 2
#define T_ML 2
#define NW1 0
#define W1E 16
#define NTW 1
#define NTF 1
#define NTX 0
#define TLBN 16
#define EARLY 1
#define AR5 -1
#define RW 16
#define RP 16
#define RQ RW
#define BAR3 1
#define BAR4 2
#define GAP 3
#define LA0 16
#define LA1 16
#define LA2 28
#define LA3 28
#define GAP2 2
#define MG 1
#define CL0 17
#define CLG 32
#define SP 8
#define PA 90
#define PF 130
#define PE 150
#define ESM 1
#define OUTST 0
#define OWN 2
#define OWNM 0
#define PREM 0
#define CA 30
#define CG 0
#define CE 0
#define CSP 3
#define NWA 8
#define RTP 1
#define RTK 14
#define POST 0
#define F3 6
#define CPOS 0
#define YCE 3
#define W5D 1
#define FAE 0
#define FLT 0
#define EPS_ 2
#define NCR 1
#define PRP 1
#define PPR 31
#define SLPR 0
#define RORD 0
#define CS0 0
#define PFM 1
#define CB 0
#define ZI 1
#define KVF 1
#define XST 2
#define BS0 0
#define BS1 0
#define BS2 0
#define BS3 0
#define PRI 0
#define AKP 1
#define CPRI 0
#define XW6 1
#define BLK 0
#define EML 0
#define TCH 0
#define TCP 1
#define LA4 99
#define TW 1
#define XO 0
#define PFN 20
#define XT 0
#define BIN 7
#define CF0 22
#define NCG 2
#define KPR 1
#define LAY 0
#define WPA 0
#define WPB 0
#define WF0 16
#define WF1 98
#define WUA 2
#define WUB 100
#define XSR 8
#define NWB 21
#define W1F 1
#define U2W 6
#define R2P 2
#define F3K 15
#define DEF0 0
#define CLOOK 8
#define CPRE 6
#define CBAR 20
#define CFL 12
#define NTK 3
#define PFW 7
#define SLP0 4
#define SLP1 16
#define SLP2 0
#define SLP3 0
#define SLP4 0
#define SLP5 0

constexpr int L_X = 0, L_H = 73728, L_HS = 147456, L_ST = 157760, L_MISC = L_ST + 2048, L_C9 = L_MISC + 64, L_BYTES = L_C9 + 512;
constexpr int L_XH = 0, L_RT = 36864;
constexpr int CU_SLOT = 160 + 4096, CF_SLOT = 96 + 4096, CX_SLOT = 19 * 256;
constexpr int L_CU = L_H, L_CF = L_CU + 4 * CU_SLOT, L_CX = L_CF + 4 * CF_SLOT;
constexpr int L_Y = L_H, L_B2 = L_H, L_A9B = L_H + 36992;
static_assert(L_CX + 8 * CX_SLOT <= L_ST && L_BYTES <= 163840 && L_A9B + 32768 <= L_HS, "lds map");

DEV unsigned pk(float a, float b) { f32x2 v = {a, b}; return __builtin_bit_cast(unsigned, __builtin_convertvector(v, bf16x2)); }
DEV float lo(unsigned u) { return __builtin_bit_cast(float, u << 16); }
DEV float hi(unsigned u) { return __builtin_bit_cast(float, u & 0xffff0000u); }
DEV float asf(unsigned u) { return __builtin_bit_cast(float, u); }
DEV unsigned asu(float f) { return __builtin_bit_cast(unsigned, f); }
DEV float dot2(unsigned a, unsigned b, float c) {
  return __builtin_amdgcn_fdot2_f32_bf16(__builtin_bit_cast(bf16x2, a), __builtin_bit_cast(bf16x2, b), c, false);
}
DEV f32x4 mfma16(v4u a, v4u b, f32x4 c) {
  return __builtin_amdgcn_mfma_f32_16x16x32_bf16(__builtin_bit_cast(bf16x8, a), __builtin_bit_cast(bf16x8, b), c, 0, 0, 0);
}
DEV f32x16 mfma32(v4u a, v4u b, f32x16 c) {
  return __builtin_amdgcn_mfma_f32_32x32x16_bf16(__builtin_bit_cast(bf16x8, a), __builtin_bit_cast(bf16x8, b), c, 0, 0, 0);
}
DEV v2u trrd(LAS char* p) { return __builtin_bit_cast(v2u, __builtin_amdgcn_ds_read_tr16_b64_v4i16((LAS s16x4*)p)); }
DEV v4u ldws(rsrc_t rs, int voff, int soff) { return __builtin_amdgcn_raw_buffer_load_b128(rs, voff, soff, 16); }
DEV void stws(rsrc_t rs, v4u v, int voff, int soff) {
  __builtin_amdgcn_raw_buffer_store_b128(v, rs, voff, soff, 16);
  asm volatile("s_nop 1" :: "v"(v));
}
DEV void bar() { asm volatile("s_waitcnt lgkmcnt(0)\n\ts_barrier" ::: "memory"); }
template <int K> DEV void bark() { asm volatile("s_waitcnt lgkmcnt(0)\n\ts_barrier ; %0" :: "n"(K) : "memory"); }
DEV void drain() { asm volatile("s_waitcnt vmcnt(0)" ::: "memory"); }
DEV void swap16(unsigned& a, unsigned& b) { auto r = __builtin_amdgcn_permlane16_swap(a, b, false, false); unsigned x = r[0], y = r[1]; a = x; b = y; }
DEV void swap32(unsigned& a, unsigned& b) { auto r = __builtin_amdgcn_permlane32_swap(a, b, false, false); unsigned x = r[0], y = r[1]; a = x; b = y; }
template <int OFF = 0, int NT = 0> DEV void glds(const void* base, unsigned voff, int ldsaddr) {
  if constexpr (NT) asm volatile("s_nop 0\n\tglobal_load_lds_dwordx4 %0, %1 offset:%3 nt" :: "v"(voff), "s"(base), "{m0}"(ldsaddr), "n"(OFF) : "memory");
  else asm volatile("s_nop 0\n\tglobal_load_lds_dwordx4 %0, %1 offset:%3" :: "v"(voff), "s"(base), "{m0}"(ldsaddr), "n"(OFF) : "memory");
}
DEV void touch(const void* p, int ldsaddr) { asm volatile("s_nop 0\n\tglobal_load_lds_dword %0, off" :: "v"(p), "{m0}"(ldsaddr) : "memory"); }
template <int NT, class T> DEV T ldnt(const T* p) { if constexpr (NT) return __builtin_nontemporal_load(p); else return *p; }
DEV int lane() { int l; asm volatile("v_mbcnt_lo_u32_b32 %0, -1, 0\n\tv_mbcnt_hi_u32_b32 %0, -1, %0" : "=v"(l)); return l; }
template <class T> DEV void pin(T& v) { asm volatile("" : "+v"(v)); }
DEV unsigned mx4(unsigned m, v4u v) { m = max(max(m, v.x), v.y); return max(max(m, v.z), v.w); }
template <int N> DEV void vmwait() { asm volatile("s_waitcnt vmcnt(%0)" :: "n"(N) : "memory"); }
DEV float gelu_ln(float y, f32x4 c) {
  const float p = __builtin_fmaf(y * y, c.y, c.x);
  const float e = __builtin_amdgcn_exp2f(y * p);
  return y * __builtin_amdgcn_rcpf(__builtin_fmaf(e, c.z, c.z));
}
DEV f32x4 gelu_consts(float r, float rinv) {
  const float c0 = -2.0f * 0.7978845608028654f * 1.4426950408889634f, c1 = c0 * 0.044715f;
  return f32x4{r * c0, r * r * r * c1, rinv, 0.f};
}
DEV float stat_reduce(float s, float ss) {
  unsigned a = asu(s), b = asu(ss);
  swap16(a, b);
  float t = asf(a) + asf(b);
  unsigned c = asu(t), d = c;
  swap32(c, d);
  return asf(c) + asf(d);
}
DEV void stat_acc(v4u v, float& s, float& ss) {
  const unsigned one = 0x3f803f80u;
  s = dot2(v.x, one, s); ss = dot2(v.x, v.x, ss); s = dot2(v.y, one, s); ss = dot2(v.y, v.y, ss);
  s = dot2(v.z, one, s); ss = dot2(v.z, v.z, ss); s = dot2(v.w, one, s); ss = dot2(v.w, v.w, ss);
}
DEV void stat_mm(v4u v, f32x4& s, f32x4& ss) { s = mfma16(v4u{0x3f803f80u, 0x3f803f80u, 0x3f803f80u, 0x3f803f80u}, v, s); ss = mfma16(v, v, ss); }
DEV void stat_put(LAS char* p, f32x4 s, f32x4 ss, int l) {
  const float a = l & 1 ? ss.y : ss.x, c = l & 1 ? ss.w : ss.z;
  if ((l >> 4) == ((l & 15) >> 2)) *(LAS f32x2*)p = f32x2{s.x, l & 2 ? c : a};
}
constexpr int pairbase_c(int g) { return 144 * g + ((g & 1) << 4); }
DEV int pairbase(int g) { return 144 * g + ((g & 1) << 4); }
DEV int singlebase(int g) { return 144 * g + ((g & 1) ? 0 : 128); }
DEV int frag_off(int row, int colpiece) { return (((row >> 4) * KS + (colpiece >> 2)) * 64 + (colpiece & 3) * 16 + (row & 15)) * 16; }

DEV v4u load_flags(rsrc_t rs, int stage, int b, int l) { return ldws(rs, l * 16, O_FLAG + stage * 8192 + (b & 7) * 1024); }
DEV void load_flags_async(v4u& f, rsrc_t rs, int stage, int b, int l) {
  asm volatile("buffer_load_dwordx4 %0, %1, %2, %3 offen sc1" : "=v"(f) : "v"(l * 16), "s"(rs), "s"(O_FLAG + stage * 8192 + (b & 7) * 1024) : "memory");
}
template <int N> DEV void flags_ready(v4u& f) { asm volatile("s_waitcnt vmcnt(%1)" : "+v"(f) : "n"(N) : "memory"); }
DEV uint64_t flags_bad(v4u f, unsigned epoch) { return __builtin_amdgcn_ballot_w64(((f.x ^ epoch) | (f.y ^ epoch) | (f.z ^ epoch) | (f.w ^ epoch)) != 0); }
DEV void wait_flags(rsrc_t rs, v4u f, int stage, int b, int l, unsigned epoch, uint64_t need, int& alive) {
  auto bad = [&]() INL { return flags_bad(f, epoch) & need; };
  uint64_t any = bad();
#pragma nounroll
  while (any != 0 && alive > 0) {
    asm volatile("s_sleep 2" ::: "memory");
    f = load_flags(rs, stage, b, l);
    --alive;
    any = bad();
  }
  asm volatile("" ::: "memory");
}
DEV void publish_flag(rsrc_t rs, int stage, int id, int l, unsigned epoch) {
  if (l < 8) __builtin_amdgcn_raw_buffer_store_b32(epoch, rs, l * 1024 + id * 4, O_FLAG + stage * 8192, 16);
}
template <int N, class OFF, class USE>
DEV void settle(rsrc_t rs, v4u (&d)[N], OFF off, USE use, int& alive) {
  unsigned m = 0;
#pragma unroll
  for (int i = 0; i < N; i++) { m = mx4(m, d[i]); use(i, d[i]); }
  uint64_t any = __builtin_amdgcn_ballot_w64(m == SENT);
#pragma nounroll
  while (any != 0 && alive > 0) {
    asm volatile("" ::: "memory");
    if (m == SENT) {
#pragma unroll
      for (int i = 0; i < N; i++) d[i] = off(i);
      m = 0;
#pragma unroll
      for (int i = 0; i < N; i++) { m = mx4(m, d[i]); use(i, d[i]); }
    }
    --alive;
    any = __builtin_amdgcn_ballot_w64(m == SENT);
  }
}

template <int N, class OFF, class USE, class BAD>
DEV void settle_each(v4u (&d)[N], OFF off, USE use, BAD isbad, int& alive) {
  auto pass = [&]() INL {
    uint64_t left = 0;
#pragma unroll
    for (int i = 0; i < N; i++) { left |= __builtin_amdgcn_ballot_w64(isbad(d[i])); use(i, d[i]); }
    return left;
  };
  uint64_t left = pass();
#pragma nounroll
  while (left != 0 && alive > 0) {
    asm volatile("" ::: "memory");
    __builtin_amdgcn_s_sleep(SLPR);
#pragma unroll
    for (int i = 0; i < N; i++) { pin(d[i]); if (isbad(d[i])) d[i] = off(i); }
    --alive;
    left = pass();
  }
}
template <int SITE, int N, class OFF, class USE>
DEV void settle_s(rsrc_t rs, v4u (&d)[N], OFF off, USE use, int& alive) {
  if constexpr (PPR >> SITE & 1) settle_each<N>(d, off, use, [](v4u v) INL { return mx4(0, v) == SENT; }, alive); else settle<N>(rs, d, off, use, alive);
}

template <int N, class OFF, class USE>
DEV void settle_tag(rsrc_t rs, v4u (&d)[N], OFF off, USE use, unsigned tag, int& alive) {
  unsigned m = 0;
#pragma unroll
  for (int i = 0; i < N; i++) { m |= (d[i].z ^ tag) | (d[i].w ^ ~tag); use(i, d[i]); }
  uint64_t any = __builtin_amdgcn_ballot_w64(m != 0);
#pragma nounroll
  while (any != 0 && alive > 0) {
    asm volatile("" ::: "memory");
    if (m != 0) {
#pragma unroll
      for (int i = 0; i < N; i++) d[i] = off(i);
      m = 0;
#pragma unroll
      for (int i = 0; i < N; i++) { m |= (d[i].z ^ tag) | (d[i].w ^ ~tag); use(i, d[i]); }
    }
    --alive;
    any = __builtin_amdgcn_ballot_w64(m != 0);
  }
}

constexpr int slot_ks_c(int s) {
  if (s == 8) return 4;
  if (s == 9) return 13;
  const int g = (s < 8 ? s : s - 10) & 3, k0 = pairbase_c(g) >> 5;
  return k0 + (s < 4 ? 0 : s < 8 ? 2 : s < 14 ? 1 : 3);
}
constexpr uint64_t slot_tab(int s0) { uint64_t t = 0; for (int i = 0; i < 9; i++) t |= (uint64_t)slot_ks_c(s0 + i) << (5 * i); return t; }
DEV int slot_ks(int s) { return (int)((s < 9 ? slot_tab(0) >> (5 * s) : slot_tab(9) >> (5 * s - 45)) & 31); }
DEV unsigned unit_voff(int ld, int l) { return (l >> 3) * ld * 2 + (((l & 7) ^ ((l >> 3) & 2)) << 4); }
DEV void unit_load(const u16* p, int ld, unsigned voff, int stage, int i0 = 0, int i1 = 4) {
#pragma unroll
  for (int i = i0; i < i1; i++) glds<0, NTW>(p + 8 * i * ld, voff ^ ((i & 1) << 6), stage + i * 1024);
}
DEV v4u unit_frag(LAS char* st, int byte, int l) {
  const int q = l >> 4, a = (l & 15) >> 2, sw = (a & 2) | ((q & 1) << 2);
  LAS char* p = st + (8 * q + a) * 128 + (byte ^ (sw << 4));
  const v2u x0 = trrd(p), x1 = trrd(p + 512);
  return v4u{x0.x, x0.y, x1.x, x1.y};
}
DEV void small_load(const u16* wsrc, int u, int stage, int l) { unit_load(wsrc + 32 * (u / 9) * D + 64 * (u % 9), D, unit_voff(D, l), stage); }
DEV void small_emit(LAS char* st, rsrc_t rs, int base, int u, int l) {
#pragma unroll
  for (int t = 0; t < 4; t++) stws(rs, unit_frag(st, 32 * t + 8 * (l & 3), l), l * 16, base + ((4 * (u % 9) + t) * KS + u / 9) * 1024);
}
DEV const u16* main_src(const u16* w3, const u16* w4, int n) {
  const int c = n % 9, r = (n / 9) % KS, j = (n / 162) & 7;
  return n < 1296 ? w3 + (32 * r * F + D * j + 64 * c) : w4 + ((D * j + 32 * slot_ks(r)) * D + 64 * c);
}
DEV int main_dst(int n, int i) {
  const int c = n % 9, r = (n / 9) % KS, j = (n / 162) & 7;
  const bool m3 = n < 1296;
  const int ks = 2 * c + (i >> 1), t = i & 1;
  const int g = ks < 4 ? 0 : ks < 9 ? 1 : ks < 13 ? 2 : 3, d = ks - (pairbase(g) >> 5);
  const bool single = ks == 4 || ks == 13;
  const int ww = single ? (ks == 13 ? 2 : 0) + t : g + (d & 2) * 2;
  const int ne = ww < 4 ? 3 : 2, bs = j * 1296 + (ww < 4 ? ww * 180 : 144 + ww * 144);
  const int f = (m3 ? 0 : 18 * ne + 36) + (single ? r * 3 + 2 : d & 1 ? 18 * ne + r * 2 + t : r * ne + t);
  return O_WM + (bs + f) * 1024;
}
DEV void retile(LAS char* st, rsrc_t rs, int n, int dst, int lane0, int l) {
  const int c = n % 9, bb = l & 3;
  v4u fr[4];
#pragma unroll
  for (int i = 0; i < 4; i++) fr[i] = unit_frag(st, 64 * (i >> 1) + (c == (i < 2 ? 2 : 6) ? 32 * (i & 1) + 8 * bb : 16 * bb + 8 * (i & 1)), l);
#pragma unroll
  for (int i = 0; i < 4; i++) stws(rs, fr[i], l * 16, __builtin_amdgcn_readlane(dst, lane0 + i));
}

template <bool NINE, int G2, class SIDE>
DEV void small_gemm_t(const v4u (&wa)[21], LAS char* p0, LAS char* p1, LAS char* a9, f32x4& a0, f32x4& a1, f32x4& a2, SIDE side) {
  v4u b0[3], b1[3], wn[3];
  auto rd = [&](int ks) INL {
    b0[ks % 3] = *(LAS v4u*)(p0 + ks * 1024); b1[ks % 3] = *(LAS v4u*)(p1 + ks * 1024);
    if (NINE) wn[ks % 3] = *(LAS v4u*)(a9 + (G2 ? ks * 1152 : (ks & 7) * 4096 + (ks >> 3) * 1024));
  };
  if (G2 && (BIN & 2)) bark<40 + NINE>();
  if (!G2 && (BIN & 8)) { bark<30 + NINE>(); side(IC<-1 - NINE>{}); }
  rd(0); rd(1);
  SB();
  sfor<0, KS>([&](auto k_) INL {
    constexpr int ks = decltype(k_)::v;
    if constexpr (ks + 2 < KS) rd(ks + 2);
    a0 = mfma16(wa[ks], b0[ks % 3], a0); a1 = mfma16(wa[ks], b1[ks % 3], a1);
    if constexpr (NINE) a2 = mfma16(wn[ks % 3], b0[ks % 3], a2);
    side(k_);
    SB();
  });
}
template <int G2, class SIDE>
DEV void small_gemm(const v4u (&wa)[21], LAS char* bimg, LAS char* a9, int w, int l, f32x4& a0, f32x4& a1, f32x4& a2, SIDE side) {
  LAS char* p = bimg + l * 16;
  if (w >= (G2 ? 4 : 2)) small_gemm_t<false, G2>(wa, p, p + KS * 1024, a9, a0, a1, a2, side);
  else {
    const int m = w & 1;
    f32x4 x = m ? a1 : a0, y = m ? a0 : a1;
    small_gemm_t<true, G2>(wa, p + m * KS * 1024, p + (m ^ 1) * KS * 1024, a9, x, y, a2, side);
    a0 = m ? y : x; a1 = m ? x : y;
  }
}
DEV int ks9(int w, int i) { return i < 2 || w < 2 ? w + 8 * i : w + 8; }
DEV v4u load_gw(rsrc_t rs, int base, int g, int w, int l, int i) {
  return i < KS ? ldws(rs, l * 16 + (i & 3) * 1024, base + ((9 * g + w) * KS + (i & ~3)) * 1024)
                : ldws(rs, l * 16, base + ((9 * g + 8) * KS + ks9(w, i - KS)) * 1024);
}
DEV v4u piece2(f32x4 a, f32x4 b) {
  unsigned a0 = pk(a.x, a.y), a1 = pk(a.z, a.w), b0 = pk(b.x, b.y), b1 = pk(b.z, b.w);
  swap16(a0, b0); swap16(a1, b1);
  return v4u{a0, a1, b0, b1};
}

template <int B0, int B1> struct Conv {
  struct Plan { int n, blk[104], m[104], nf, fr[48], use[48], issued[105], fin[32]; };
  static constexpr Plan plan() {
    Plan p{};
    bool seen[48] = {};
    for (int m = 0; m < 16; m++)
      for (int b = B0 > 2 * m ? B0 : 2 * m; b <= B1; b++) {
        for (int k = 0; k < 2; k++) { const int f = k ? 16 + b - 2 * m : m; if (!seen[f]) { seen[f] = true; p.fr[p.nf] = f; p.use[p.nf++] = p.n; } }
        p.fin[b] = p.n; p.blk[p.n] = b; p.m[p.n++] = m;
      }
    int i = CPRE;
    for (int n = 0; n < p.n; n++) { p.issued[n] = i; if (i < p.nf && p.use[i] <= n + CLOOK) i++; }
    p.issued[p.n] = i;
    return p;
  }
};
DEV float ror1(float v) { return asf(__builtin_amdgcn_update_dpp(0, asu(v), 0x13c, 0xf, 0xf, false)); }
template <int B0, int B1, class SIDE, class EMIT>
DEV void conv_unit(LAS char* ap, LAS char* bp, LAS char* zp, int l, SIDE side, EMIT emit) {
  constexpr typename Conv<B0, B1>::Plan P = Conv<B0, B1>::plan();
  constexpr int O0 = B0 ? B0 + 1 : 0;
  v4u fr[48];
  f32x4 acc[32];
  if (BIN & 1) bark<B0>();
  auto rd = [&](int f) INL {
    const v2u a = trrd(f < 16 ? ap + 256 * f : bp + 128 * (f - 16) + 32), c = trrd(f < 16 ? ap + 256 * f + 32 : bp + 128 * (f - 16));
    fr[f] = v4u{a.x, a.y, c.x, c.y};
  };
  auto fold = [&](auto b_) INL {
    constexpr int b = decltype(b_)::v;
    f32x4 pv = {0, 0, 0, 0};
    if constexpr (b > 0) pv = acc[b - 1];
    const float x3 = l >= 61 ? pv.w : acc[b].w, x2 = l >= 62 ? pv.z : acc[b].z, x1 = l == 63 ? pv.y : acc[b].y;
    *(LAS float*)(zp + (b - O0) * 256) = ror1(ror1(ror1(x3) + x2) + x1) + acc[b].x;
  };
#pragma unroll
  for (int i = 0; i < CPRE; i++) rd(P.fr[i]);
  SB();
  sfor<0, P.n>([&](auto n_) INL {
    constexpr int n = decltype(n_)::v, b = P.blk[n], m = P.m[n];
    acc[b] = mfma16(fr[m], fr[16 + b - 2 * m], m ? acc[b] : f32x4{0, 0, 0, 0});
    if constexpr (P.issued[n + 1] > P.issued[n]) rd(P.fr[P.issued[n]]);
    sfor<O0, B1 + 1>([&](auto b_) INL { if constexpr (P.fin[decltype(b_)::v] + 3 == n) fold(b_); });
    side(n_, IC<P.n - n>{});
    if constexpr (CE && B0 == 0 && P.n - n == CE) emit(IC<O0>{}, IC<B1>{}, IC<0>{});
    SB();
  });
  sfor<O0, B1 + 1>([&](auto b_) INL { if constexpr (P.fin[decltype(b_)::v] + 3 >= P.n) fold(b_); });
  side(IC<P.n>{}, IC<0>{});
  sfor<(CE && B0 == 0), (B1 - O0) / 8 + 1>([&](auto k_) INL { emit(IC<O0>{}, IC<B1>{}, k_); });
}

template <int NE> struct Seg {
  static constexpr int TOT = 36 * (NE + 2);
  static constexpr int steps(int s) { return s == 2 ? 10 : s == 3 ? 8 : 18; }
  static constexpr int nt(int s) { return s == 1 || s == 4 ? 2 : NE; }
  static constexpr int base(int s) { int b = 0; for (int i = 0; i < s; i++) b += steps(i) * nt(i); return b; }
  struct Plan { int issued[4 * TOT + 1], end; };
  static constexpr Plan plan() {
    Plan p{};
    constexpr int cap[5] = {LA0, LA1, LA2, LA3, LA4};
    int n = RW, last = -99, U = 0;
    for (int s = 0; s < 5; s++)
      for (int u = 0; u < 4 * steps(s) * nt(s); u++, U++) {
        const int k = u % (4 * nt(s)), done = base(s) + u / (4 * nt(s)) * nt(s) + (k < 3 * nt(s) ? 0 : k + 1 - 3 * nt(s));
        p.issued[U] = n;
        if (n < TOT && n - done < cap[s] && U - last >= (s == 2 ? GAP2 : GAP)) { n++; last = U; }
      }
    p.issued[U] = n; p.end = last + 1;
    return p;
  }
};
constexpr int late_stores(int u1) { int n = 0; for (int u = 3; u < u1; u += 2) n += (u / 2 - 1) % (18 * MG) / MG == 17; return n; }
constexpr int NP = OWN == 2 ? 7 : 8;
DEV int part_base(int j, int i) { return O_PART + (OWN == 2 ? (j + 1 + i) & 7 : i) * ACT; }
DEV v4u ring_load(rsrc_t rs, int sbase, int l16, int f) { return ldws(rs, l16 + (f & 3) * 1024, sbase + (f & ~3) * 1024); }

DEV unsigned warm(rsrc_t rs, int sbase, int l, int r, int f0, int f1) {
  const int f = f0 + 2 * l + (r >> 3);
  return __builtin_amdgcn_raw_buffer_load_b32(rs, (f * 1024 + (r & 7) * 128) | (f < f1 ? 0 : OOB), sbase, 16);
}
template <int NE>
DEV void mlp_body(LAS char* lds, rsrc_t rs, const v4u (&ring)[RW], const v4u (&tx)[9], int sbase, int b, int j, int tile, int w, unsigned epoch, v4u& fl,
                  v4u (&pe)[NP], int& eo, int fi, unsigned wua, unsigned wub
) {
  using G = Seg<NE>;
  constexpr typename G::Plan PL = G::plan();
  constexpr int U4 = 4 * G::base(4), ES = ESM && PL.end > U4 ? PL.end - U4 : 0, SQ = SP * 3 / NE, EL = ES + (2 * NE - 1) * SQ;
  const int g = w & 3, odd = g & 1;
  const int l16 = lane() * 16;
  LAS char* const bx = lds + L_X + l16;
  LAS char* const bh = lds + L_H + l16;
  auto bptr = [&](int J) INL {
    const int r = J - 216, T = J < 216 ? J >> 2 : r % (18 * MG) / MG, m = J < 216 ? J & 3 : r / (18 * MG) * MG + r % MG;
    return (LAS v4u*)((J < 144 ? bx : bh) + (T % 18) * 4096 + m * 1024);
  };
  f32x4 acc[4][NE][4];
  v4u bf[4], fr[G::TOT];
  if (BIN & 4) bark<50 + NE>();
#pragma unroll
  for (int m = 0; m < 4; m++) bf[m] = *bptr(m);
#pragma unroll
  for (int i = 0; i < RW; i++) fr[i] = ring[i];
  float t0 = 0, t1 = 0, qa = 0;
  f32x4 sq4 = {0, 0, 0, 0};
  f32x4 gc[2];
  unsigned go[4], cnt = 0;
  LAS char *gcp = lds, *hp = lds, *hs = lds;
  const v4u sent = {SENT, SENT, SENT, SENT};
  auto gelu_chunk = [&](auto c_, auto ph_, auto per_) INL {
    constexpr int c = decltype(c_)::v, ph = decltype(ph_)::v, PER = decltype(per_)::v, P = c / 10, k = c % 10, m = P / PER, e = 2 * (P % PER);
    const f32x4 av = acc[ph][e >> 2][m], kc = gc[m & 1];
    const float y0 = av[e & 3], y1 = av[(e & 3) + 1];
    if constexpr (k == 0) { t0 = y0 * y0; pin(t0); t1 = y1 * y1; }
    if constexpr (k == 1) { t0 = __builtin_fmaf(t0, kc.y, kc.x); pin(t0); t1 = __builtin_fmaf(t1, kc.y, kc.x); }
    if constexpr (k == 2 || k == 8) { t0 *= y0; pin(t0); t1 *= y1; }
    if constexpr (k == 3) t0 = __builtin_amdgcn_exp2f(t0);
    if constexpr (k == 4) t1 = __builtin_amdgcn_exp2f(t1);
    if constexpr (k == 5) { t0 = __builtin_fmaf(t0, kc.z, kc.z); pin(t0); t1 = __builtin_fmaf(t1, kc.z, kc.z); }
    if constexpr (k == 6) t0 = __builtin_amdgcn_rcpf(t0);
    if constexpr (k == 7) t1 = __builtin_amdgcn_rcpf(t1);
    if constexpr (k == 9) {
      go[(e >> 1) & 3] = pk(t0, t1);
      if constexpr (e == 6) *(LAS v4u*)(hp + m * 1024) = v4u{go[0], go[1], go[2], go[3]};
      if constexpr (e == 10) *(LAS v2u*)(hs + m * 1024) = v2u{go[0], go[1]};
      if constexpr (e == 0 && m < 3) gc[(m + 1) & 1] = *(LAS f32x4*)(gcp + (m + 1) * 256);
    }
  };
  auto gelu_setup = [&](int slot, bool single) INL {
    gcp = lds + L_ST + (l16 & 255);
    gc[0] = *(LAS f32x4*)gcp;
    hp = bh + slot * 4096;
    if (single) hs = lds + L_H + (8 + (g >> 1)) * 4096 + (2 * odd + (l16 >> 9)) * 256 + (l16 & 255) + ((l16 >> 5) & 8);
  };
  const int pdst = O_PART + j * ACT + 4 * tile * KS * 1024, ks0 = (pairbase(g) >> 5) + (NE == 3 ? 0 : 2);
  const int og = OWNM ? j >> 1 : j & 3, op = OWNM ? j & 1 : j >> 2;
  const int ob[2] = {OWN == 2 && g == og && op == 0 ? OOB : 0, OWN == 2 && g == og && op == 1 ? OOB : 0};
  auto store_pair = [&](int pp, int m) INL {
    f32x4 a = acc[2 + pp][0][m], c = acc[2 + pp][1][m];
    const v4u o = {pk(a.x, a.y), pk(a.z, a.w), pk(c.x, c.y), pk(c.z, c.w)};
    if (OWN == 2) *(LAS v4u*)(bx + (m * KS + ks0 + pp) * 1024) = o;
    stws(rs, o, l16 | ob[m >> 1], pdst + (m * KS + ks0 + pp) * 1024);
  };
  constexpr int UC0 = 4 * G::base(1) + CL0, UCE = UC0 + 3 * CLG;
  sfor<0, 5>([&](auto s_) INL {
    constexpr int s = decltype(s_)::v, nst = G::steps(s), n = G::nt(s), ph = s < 3 ? s : s - 1;
    constexpr int NCH = s == 1 ? 80 * NE : s == 2 ? 160 : 0;
    constexpr int SPAN = (nst - 1) * 4 * n - 2;
    sfor<0, nst>([&](auto st_c) INL {
      constexpr int sq = decltype(st_c)::v;
      constexpr bool last = sq == nst - 1;
      if constexpr (last && s < 3) bar();
      if constexpr ((BAR3 && last && s == 3) || (s == 4 && BAR4 && sq % (BAR4 + !BAR4) == 0 && sq > 0)) asm volatile("s_barrier");
      constexpr int BS = s == 0 ? BS0 : s == 1 ? BS1 : s == 2 ? BS2 : s == 3 ? BS3 : 0;
      if constexpr (BS && sq % (BS + !BS) == 0 && sq > 0 && !last) asm volatile("s_barrier");
      if constexpr (PRI && NE == 3 && sq == 0) __builtin_amdgcn_s_setprio(PRI >> s & 1);
      if constexpr (last && s < 2) gelu_setup(10 * s + w, s == 0 && NE == 3);
      sfor<0, 4 * n>([&](auto k_) INL {
        constexpr int k = decltype(k_)::v, i = k % n, u = 4 * n * sq + k, U = 4 * G::base(s) + u, r = u / n;
        constexpr int st = s < 4 ? sq : r % (18 * MG) / MG, m = s < 4 ? k / n : r / (18 * MG) * MG + r % MG;
        constexpr int J = (s < 3 ? 72 * s : s == 3 ? 184 : 216) + r, f = G::base(s) + st * n + i;
        static_assert(f < PL.issued[U], "plan");
        acc[ph][i][m] = mfma16(fr[f], bf[J & 3], st == 0 && s != 3 ? f32x4{0, 0, 0, 0} : acc[ph][i][m]);
        if constexpr (i == n - 1 && J + 4 < 288) bf[J & 3] = *bptr(J + 4);
        if constexpr (PL.issued[U + 1] > PL.issued[U]) fr[PL.issued[U]] = ring_load(rs, sbase, l16, PL.issued[U]);
        if constexpr (WPA == 4 && U == WUA * NE / 2) wua = warm(rs, sbase, l16 >> 4, tile & 15, WF0, WF1 * (NE + 2) / 5);
        if constexpr (WPA == 4 && U == (WUA + 80) * NE / 2) asm volatile("" :: "v"(wua));
        if constexpr (WPB == 4 && U == WUB * NE / 2) wub = warm(rs, sbase, l16 >> 4, tile & 15, WF1 * (NE + 2) / 5, G::TOT);
        if constexpr (WPB == 4 && U == (WUB + 80) * NE / 2) asm volatile("" :: "v"(wub));
        if constexpr (WPA == 3 && U == 40 * NE) asm volatile("" :: "v"(wua));
        if constexpr (WPB == 3 && U == 40 * NE) asm volatile("" :: "v"(wub));
        if constexpr (NCH > 0 && u < SPAN) sfor<u * NCH / SPAN, (u + 1) * NCH / SPAN>([&](auto c_) INL { gelu_chunk(c_, IC<s - 1>{}, IC<(s == 1 ? 2 * NE : 4)>{}); });
        if constexpr (s == 0 && i == 1 && r < 9) sq4 = mfma16(tx[r], tx[r], sq4);
        if constexpr (s == 0 && i == 1 && r == 11) {
          const float a = l16 & 16 ? sq4.y : sq4.x, c = l16 & 16 ? sq4.w : sq4.z;
          qa = l16 & 32 ? c : a;
          if ((l16 >> 8) != ((l16 >> 6) & 3)) qa = 0.f;
        }
        if constexpr (s == 0 && i == 1 && r == 12) {
          qa += asf(__builtin_amdgcn_update_dpp(0, asu(qa), 0x128, 0xf, 0xf, false));
          unsigned a = asu(qa), c = a;
          swap16(a, c);
          qa = asf(a) + asf(c);
        }
        if constexpr (s == 0 && i == 1 && r == 13) {
          unsigned a = asu(qa), c = a;
          swap32(a, c);
          qa = (asf(a) + asf(c)) * (1.0f / D) + EPS;
        }
        if constexpr (s == 0 && i == 1 && r == 14) { const float rr = __builtin_amdgcn_rsqf(qa); *(LAS f32x4*)(lds + L_ST + w * 128 + (l16 & 127)) = gelu_consts(rr, qa * rr); }
        if constexpr (U >= UC0 && U <= UCE && (U - UC0) % CLG == 0)
          stws(rs, sent, l16 | ob[(U - UC0) / CLG >> 1], pdst + ((U - UC0) / CLG * KS + ks0 + 1) * 1024);
        if constexpr (s == 4 && u == PA) {
          static_assert(PA > EL && PF > PA + 5 && PE > PF, "order");
          vmwait<PL.issued[U4 + PA + 1] - PL.issued[U4 + EL + 1] + late_stores(PA) - late_stores(EL)>();
          if (l16 == 0) cnt = __hip_atomic_fetch_add((LAS unsigned*)(lds + L_MISC + 8), 1u, __ATOMIC_RELAXED, __HIP_MEMORY_SCOPE_WORKGROUP);
        }
        if constexpr (s == 4 && u == PA + 5) { if (__builtin_amdgcn_readfirstlane(cnt) == 7) publish_flag(rs, 2, 8 * tile + j, l16 >> 4, epoch); }
        if constexpr (s == 4 && u == PF) fl = load_flags(rs, 2, b, fi + ((l16 >> 4) & 1));
        if constexpr (s == 4 && u == PE) { if (flags_bad(fl, epoch)) eo |= OOB; }
        if constexpr (s == 4 && u >= ES && (u - ES) % SQ == 0 && (u - ES) / SQ < 2 * NE) {
          constexpr int e = (u - ES) / SQ;
          if constexpr (e < 4) store_pair(0, e);
          else {
          {
            const int q = l16 >> 8;
            const v4u o = piece2(acc[2][NE - 1][2 * e - 8], acc[2][NE - 1][2 * e - 7]);
            const int fo = frag_off(16 * (2 * e - 8 + (q & 1)) + ((l16 >> 4) & 15), (singlebase(g) >> 3) + (q >> 1));
            if (OWN == 2) *(LAS v4u*)(lds + L_X + fo) = o;
            stws(rs, o, fo | ob[e - 4], pdst);
          } }
        }
        if constexpr (s == 4 && u >= 3 && i == 1) {
          constexpr int rp = r - 1;
          if constexpr (rp % (18 * MG) / MG == 17) store_pair(1, rp / (18 * MG) * MG + rp % MG);
        }
        if constexpr (s == 4 && u >= PE && u < PE + 2 * NP && !(u & 1)) pe[(u - PE) / 2] = ldws(rs, eo, part_base(j, (u - PE) / 2));
        SB();
      });
    });
  });
  store_pair(1, 3);
  if constexpr (PE >= 144 && !TW) {
    if (flags_bad(fl, epoch)) eo |= OOB;
#pragma unroll
    for (int i = 0; i < NP; i++) pe[i] = ldws(rs, eo, part_base(j, i));
  }
}

__global__ __launch_bounds__(512, 2) void mega(const u16* __restrict__ x, const u16* __restrict__ kin, const u16* __restrict__ vin,
                                               const u16* __restrict__ sk, const u16* __restrict__ filt, const u16* __restrict__ Dv,
                                               const u16* __restrict__ w1, const u16* __restrict__ w2, const u16* __restrict__ w3,
                                               const u16* __restrict__ w4, char* ws, u16* out) {
  __shared__ __attribute__((aligned(16))) char smem[L_BYTES];
  LAS char* lds = (LAS char*)smem;
  const int ldsb = (int)(uintptr_t)lds;
  const int tid = threadIdx.x, l = tid & 63, w = __builtin_amdgcn_readfirstlane(tid >> 6);
  int b = blockIdx.x;
  unsigned pf;
  {
    uint64_t pc, sv;
    const unsigned i = tid - 64 * PFW;
    asm volatile("s_getpc_b64 %1\n.Lpf_beg:\n\tv_cmp_gt_u32 vcc, %5, %3\n\ts_and_saveexec_b64 %2, vcc\n\tv_min_u32 %0, .Lpf_end-.Lpf_beg-4+%6, %4\n\t"
                 "global_load_dword %0, %0, %1\n\ts_mov_b64 exec, %2" : "=&v"(pf), "=&s"(pc), "=&s"(sv) : "v"(i), "v"((PFM ? i + PFN * (b >> 3) : i * 32 + (b >> 3)) * 128), "n"(PFN), "n"(LAY ? 3968 : 0) : "vcc");
  }
  if (CB || ZI) *(LAS v4u*)(lds + L_MISC) = v4u{0, 0, 0, 0};
  const rsrc_t rs = __builtin_amdgcn_make_buffer_rsrc((void*)ws, 0, WS_USED, 0x00020000);
  const v4u sent = {SENT, SENT, SENT, SENT};
  const int tb8 = 32 * (b & 7) + (b >> 3);
  const int cp = min(tid, 287), tb = 8 * tb8 + 4 * (cp & 1);
  const int kr = (8 * tb8 + (l >> 4)) * (D * 2), kc = 256 * w + 16 * (l & 15);
  unsigned k0 = kr + min(kc, D * 2 - 16), k1 = kr + min(kc ^ 128, D * 2 - 16);
  unsigned ko[6] = {(unsigned)max((int)k0 - 3 * D * 2, 0), k1 + D * 2, min(k0 + 5 * D * 2, (unsigned)ACT - 16), k0, k1 + 4 * D * 2, (unsigned)min(tid * 16, D * 8 - 16)};
#pragma unroll
  for (int i = 0; i < 6; i++) pin(ko[i]);
  asm volatile("s_waitcnt lgkmcnt(0)" :: "s"(x), "s"(kin), "s"(vin), "s"(sk), "s"(filt), "s"(Dv), "s"(w1), "s"(w2), "s"(w3), "s"(w4), "s"(ws), "s"(out) : "memory");
  unsigned ew;
  if (EPS_ == 2) asm volatile("s_load_dword %0, %1, %2" : "=s"(ew) : "s"(ws), "n"(O_EW));
  auto kv_load = [&]() INL {
#pragma unroll
    for (int i = 0; i < 6; i++) {
      const int la = ldsb + L_XH + w * 6144 + i * 1024;
      if (i < 3) glds<0, (NTK & 1)>(kin, ko[i], la); else if (i < 5) glds<0, (NTK & 2)>(vin, ko[i], la); else glds(sk, ko[i], la);
    }
  };
  int w5 = w;
  if (KVF) { if (w < 5) kv_load(); asm volatile("" : "+s"(w5)); }
  if (!CB && EARLY && w5 == 5 && b < 162) { small_load(w1, b, ldsb + L_CX, l); small_load(w2, b, ldsb + L_CX + 4096, l); }
  const int tok = l & 15, q = l >> 4;
  int alive = SPIN_CAP;

  unsigned epoch;
  auto chan = [&](int p) INL { return 4 * (p % 144) + p / 144; };
  const int rt = w & 1, kq = w >> 1;
  bool conv; int pb, jj, tile64, g, t32, t0, un, un2, type, slot, cpart;
  auto bar0 = [&]() INL {
    if (!CB) asm volatile("s_barrier" : "+s"(b) :: "memory");
    const int xcd = b & 7, rk = b >> 3, ci = 27 * xcd + rk;
    conv = b < NCONV;
    pb = 8 * (ci / 3) + 2 * (ci % 3);
    jj = JX * (xcd % (8 / JX)) + rk % JX; tile64 = (32 / JX) * (xcd / (8 / JX)) + rk / JX;
    g = !OWN ? rk & 3 : OWNM ? jj >> 1 : jj & 3; t32 = !OWN ? 8 * xcd + (rk >> 2) : 2 * tile64 + (OWNM ? jj & 1 : jj >> 2); t0 = 32 * t32;
    un = 8 * b + w;
    un2 = !conv ? un + 320 : w == U2W ? 2368 + b : w == 5 && (b >> 3) == 21 ? 2416 + b : -1;
    const int part = ci % 3, u24 = 8 * part + w, c8 = u24 / 3, r3 = u24 % 3;
    type = r3; cpart = part;
    slot = c8 - 2 * part;
  };
  const unsigned xvo = (l >> 3) * D * 2 + (((l & 7) ^ ((l >> 3) & 6)) << 4);
  auto stage_x = [&](int rg, auto c_) INL { constexpr int c = decltype(c_)::v; glds<128 * c, NTX>(x + (t0 + 8 * rg) * D, xvo, ldsb + L_XH + rg * 9216 + c * 896); };
  auto stage_x5 = [&](auto i_) INL {
    constexpr int i = decltype(i_)::v;
    if (i < 4 || !(w & 1)) glds<128 * i, NTX>(x + (t0 + 8 * (w >> 1)) * D + 320 * (w & 1), xvo, ldsb + L_XH + (w >> 1) * 9216 + (w & 1) * 5120 + i * 896);
  };
  auto side = [&](int when) INL {
    if (when == T_FILT && conv) {
#pragma unroll
      for (int i = 0; i < 2; i++) {
        const int sl = 2 * (w - 6) + i;
        sfor<0, 4>([&](auto p_) INL { glds<1024 * decltype(p_)::v, NTF>(filt + chan(pb + sl) * S, l * 16, ldsb + L_CF + sl * CF_SLOT + 96); });
      }
    }
    if (when == 1 && conv) {
      const int u0 = 2 * cpart - 3 * ((l >> 2) & 3), tlo = max(u0, 0), thi = min(u0 + 8, 3);
      auto edge = [&](int t) INL { return t <= 0 ? 0 : t == 1 ? 38 : t == 2 ? 52 : 64; };
      const int e0 = edge(tlo) - (l >> 4) - 32 * (w - 6), e1 = edge(thi) - (l >> 4) - 32 * (w - 6);
#pragma unroll
      for (int i = 0; i < 8; i++) stws(rs, sent, ((l >> 4) * D * 64 + (l & 15) * 16) | (4 * i >= e0 && 4 * i < e1 ? 0 : OOB), O_YV + ((32 * (w - 6) + 4 * i) * D + pb) * 64);
    }
  };
  constexpr int L_MS = T_ML == 3 ? L_CX : L_RT;
  const u16 *msrc, *msrc2; int mld, mdst; unsigned mvo;
  auto prep = [&]() INL {
    msrc = main_src(w3, w4, un); mld = un < 1296 ? F : D; mvo = unit_voff(mld, l);
    mdst = main_dst(l & 4 ? max(un2, 0) : un, l & 3);
    msrc2 = main_src(w3, w4, BLK && conv && w < 4 ? un + 4 : max(un2, 1296));
    if (BLK && conv && w >= 4) { msrc = msrc2; mld = D; mvo = unit_voff(D, l); }
    pin(mvo); pin(mdst); asm volatile("" : "+s"(msrc), "+s"(msrc2), "+s"(mld));
    if (conv) {
      if ((tid & 127) < 16) *(LAS v4u*)(lds + ((tid & 127) < 10 ? L_CU + (tid >> 7) * CU_SLOT : L_CF - 160 + (tid >> 7) * CF_SLOT) + (tid & 127) * 16) = v4u{0, 0, 0, 0};
    }
  };
  auto main_load = [&](int i0, int i1) INL { unit_load(msrc, mld, mvo, ldsb + L_MS + w * 4096, i0, i1); };
  auto st2 = [&]() INL { return conv ? L_HS + (w == 5) * 4096 : L_H + w * 4096; };
  auto load2 = [&](int i0, int i1) INL { unit_load(msrc2, D, unit_voff(D, l), ldsb + st2(), i0, i1); };
  auto clear_own = [&](auto i_) INL {
    constexpr int i = decltype(i_)::v;
    const int row = t0 + 16 * (q & 1) + tok, tt = (row + (g == 0 ? 1 : g == 1 ? -1 : g == 2 ? 2 : -2)) & (S - 1);
    v4u sv = sent; pin(sv);
    if constexpr (i < 2) stws(rs, sv, frag_off(i ? row : tt, 18 * g + 2 * w + (q >> 1)), i ? O_X1 : O_HSH);
    else if ((w & 6) == 2) stws(rs, sv, frag_off(w & 1 ? row : tt, 18 * g + 16 + (q >> 1)), w & 1 ? O_X1 : O_HSH);
  };
  auto request = [&](auto i_) INL {
    constexpr int j = decltype(i_)::v - (T_XS == 2 ? 5 : 0), i = RORD == 1 && j >= 0 && j < 7 ? (j + 3) % 7 : j;
    if constexpr (i < 0) stage_x5(i_);
    else if constexpr (i < 3) { if (!CPOS) clear_own(IC<i>{}); }
    else if constexpr (BLK && i < 7) { if (w < 4 || un2 >= 0) unit_load(msrc, mld, mvo, ldsb + (w < 4 ? L_MS + w * 4096 : st2()), i - 3, i - 2); }
    else if constexpr (BLK && i < 11) { if (w < 4) unit_load(msrc2, mld, mvo, ldsb + L_MS + (w + 4) * 4096, i - 7, i - 6); }
    else if constexpr (i < 7) { if (EML != 255 && !(EML >> w & 1)) main_load(i - 3, i - 2); }
    else if constexpr (i < 11) { if (EML != 255 && un2 >= 0 && !(EML >> w & 1)) load2(i - 7, i - 6); }
  };
  auto early = [&]() INL {
    if (!EML || !conv || !(EML >> w & 1)) drain();
    else {
      main_load(0, 4);
      if (un2 >= 0) { load2(0, 4); vmwait<8>(); } else vmwait<4>();
    }
  };
  constexpr int NY = T_XS == 2 ? 8 : 4;
  auto arrive = [&](unsigned inc) INL {
    unsigned n = 0;
    if (l == 0) n = __hip_atomic_fetch_add((LAS unsigned*)(lds + L_MISC), inc, __ATOMIC_RELAXED, __HIP_MEMORY_SCOPE_WORKGROUP);
    if (__builtin_amdgcn_readfirstlane(n) + inc == 0x305) publish_flag(rs, 0, b, l, epoch);
  };
  auto after_kv = [&]() INL {
#pragma nounroll
    while ((__builtin_amdgcn_readfirstlane(*(volatile LAS unsigned*)(lds + L_MISC)) & 255) != 5 && alive > 0) { asm volatile("s_sleep 1" ::: "memory"); --alive; }
    asm volatile("" ::: "memory");
  };
  auto get_epoch = [&]() INL {
    if (EPS_ < 2) { pin(epoch); asm volatile("" :: "v"(pf)); epoch = __builtin_amdgcn_readfirstlane(epoch) + EPOCH_STEP; }
    else { asm volatile("s_waitcnt lgkmcnt(0)\n\ts_add_u32 %0, %1, %2" : "=s"(epoch) : "s"(ew), "n"(EPOCH_STEP), "v"(pf) : "scc"); }
  };
  if (CB) {
    if (w < 5) kv_load();
    asm volatile("" : "+s"(w5));
    if (w5 == 5 && b < 162) { small_load(w1, b, ldsb + L_CX, l); small_load(w2, b, ldsb + L_CX + 4096, l); }
    asm volatile("s_barrier" : "+s"(b) :: "memory");
  }
  if (w >= 5) {
    if (!CB && !ZI) {
      *(LAS v4u*)(lds + L_MISC) = v4u{0, 0, 0, 0};
      asm volatile("s_waitcnt lgkmcnt(0)" ::: "memory");
    }
    bar0();
    auto clear_all = [&]() INL {
      const int row = t0 + 16 * (q & 1) + tok, tt = (row + (g == 0 ? 1 : g == 1 ? -1 : g == 2 ? 2 : -2)) & (S - 1);
      const int o = frag_off(w & 1 ? row : tt, 0) + (w & 1 ? O_X1 : O_HSH);
#pragma unroll
      for (int i = 0; i < 9; i++) stws(rs, sent, o + frag_off(0, 18 * g + 2 * i + (q >> 1)), 0);
    };
    auto xtouch = [&]() INL { touch(x + tb8 * 8 * D + (36 * (w - 6) + min(l, 35)) * 64, ldsb + L_ST + (w - 6) * 256); };
    if (LAY ? __builtin_expect(w > 5, 1) : w > 5) {
      if (XT == 1) xtouch();
      if (YCE == 3) side(1);
      if (XT == 2) xtouch();
      if (CPOS == 2) clear_all();
      if (FLT == 0) side(0);
      if (XT == 3) xtouch();
      if (CPOS == 3) clear_all();
      if (EPS_ < 2) epoch = __builtin_amdgcn_raw_buffer_load_b32(rs, 0, O_EW, EPS_ ? 0 : 16);
      unsigned dv;
      asm volatile("global_load_ushort %0, %1, %2" : "=v"(dv) : "v"(2 * min(chan(pb + (l & 3)), D - 1)), "s"(Dv) : "memory");
      SB();
      if (YCE == 1) side(1);
      SB();
      prep();
      if (FLT == 1) side(0);
      if (YCE == 2) side(1);
      if (!YCE) after_kv();
      if (!YCE) side(1);
      if (DEF0 || (FLT == 1 && conv)) vmwait<8>(); else early();
      pin(dv);
      get_epoch();
      *(LAS unsigned*)(lds + L_MISC + 16 + (l & 3) * 4) = (unsigned)dv << 16;
      if (!DEF0 || !conv) arrive(256);
      if (XT == 4) { after_kv(); xtouch(); }
      if (conv || !NCR) {
        if (YCE) after_kv();
        if (FLT == 2) side(0);
        if (T_XS == 1 || !conv) sfor<0, 9>([&](auto c_) INL { stage_x(w - 4, c_); });
        if (FLT) vmwait<T_XS == 1 ? 9 : 0>();
      }
    } else if (b < 184) {
      if (!EARLY && b < 162) { small_load(w1, b, ldsb + L_CX, l); small_load(w2, b, ldsb + L_CX + 4096, l); }
      if (EPS_ < 2) epoch = __builtin_amdgcn_raw_buffer_load_b32(rs, 0, O_EW, EPS_ ? 0 : 16);
      prep();
      get_epoch();
      early();
      if (W5D) { after_kv(); __builtin_amdgcn_s_sleep(W5D - 1); }
      if (b < 162) { small_emit(lds + L_CX, rs, O_W1, b, l); small_emit(lds + L_CX + 4096, rs, O_W2, b, l); }
      drain();
      arrive(256);
    } else {
      v4u m2[9];
#pragma unroll
      for (int i = 0; i < 9; i++) m2[i] = ldnt<NTW>((const v4u*)(w2 + (8 * (b - 184) + (l >> 3)) * D + 64 * i + 8 * (l & 7)));
      if (EPS_ < 2) epoch = __builtin_amdgcn_raw_buffer_load_b32(rs, 0, O_EW, EPS_ ? 0 : 16);
      float sm = 0, unused = 0;
#pragma unroll
      for (int i = 0; i < 9; i++) stat_acc(m2[i], sm, unused);
      sm += asf(__builtin_amdgcn_update_dpp(0, asu(sm), 0xb1, 0xf, 0xf, false));
      sm += asf(__builtin_amdgcn_update_dpp(0, asu(sm), 0x4e, 0xf, 0xf, false));
      sm += asf(__builtin_amdgcn_update_dpp(0, asu(sm), 0x141, 0xf, 0xf, false));
      sm *= 1.0f / D;
      const unsigned mh = pk(sm, sm);
      get_epoch();
      if ((l & 7) == 0) __builtin_amdgcn_raw_buffer_store_b32(pk(sm, sm - lo(mh)), rs, (l >> 3) * 4, O_M2 + (b - 184) * 32, 16);
      if (NCG) *(volatile LAS unsigned*)(lds + L_MISC + 12) = 1;
      prep();
      early();
      arrive(256);
    }
  } else {
    v2u kk[7], vv[4]; v4u sw[2];
    if (!CB && !KVF) kv_load();
    if (EPS_ < 2) epoch = __builtin_amdgcn_raw_buffer_load_b32(rs, 0, O_EW, EPS_ ? 0 : 16);
    bar0();
    LAS char* const kp = lds + L_XH + w * 6144 + (cp & 1) * 1024;
    const int ka = (4 * cp & 248) ^ (cp & 1) * 128;
    if (EPS_ == 2) get_epoch();
    if (PRP == 1 || (PRP == 2 && w < 4)) prep();
    if (KPR) __builtin_amdgcn_s_setprio(3);
    early();
#pragma unroll
    for (int i = 0; i < 7; i++) kk[i] = *(LAS v2u*)(kp + (i < 4 ? ka : ka ^ 128) + 256 * i);
#pragma unroll
    for (int i = 0; i < 2; i++) sw[i] = *(LAS v4u*)(lds + L_XH + w * 6144 + 5120 + (cp & 62) * 16 + 16 * i);
#pragma unroll
    for (int i = 0; i < 4; i++) vv[i] = *(LAS v2u*)(kp + ka + 3072 + 256 * i);
    if (EPS_ < 2) get_epoch();
    if (tb == 0) kk[0] = kk[1] = kk[2] = v2u{0, 0};
#pragma unroll
    for (int e = 0; e < 4; e++) {
      unsigned qk[7]; float u[4];
#pragma unroll
      for (int i = 1; i < 7; i++) { const unsigned k0 = kk[i - 1][e >> 1], k1 = kk[i][e >> 1]; qk[i] = __builtin_amdgcn_perm(k0, k1, e & 1 ? 0x07060302u : 0x05040100u); }
      const unsigned s01 = sw[e >> 1][2 * (e & 1)], s23 = sw[e >> 1][2 * (e & 1) + 1];
#pragma unroll
      for (int r = 0; r < 4; r++) { const unsigned v = vv[r][e >> 1]; u[r] = dot2(qk[r + 3], s01, dot2(qk[r + 1], s23, 0.f)) * (e & 1 ? hi(v) : lo(v)); }
      stws(rs, v4u{pk(u[0], u[1]), pk(u[2], u[3]), epoch, ~epoch}, cp * 16, O_UT + tb8 * 18432 + e * 4608);
    }
    arrive(1);
    if ((NCR ? T_XS == 1 && conv : T_XS == 1 || !conv) && w == 4) { after_kv(); sfor<0, 9>([&](auto c_) INL { stage_x(0, c_); stage_x(1, c_); }); }
    if (KPR) __builtin_amdgcn_s_setprio(0);
    if (!PRP || (PRP == 2 && w == 4)) prep();
  }
  auto uni = [&](int& v) INL { v = __builtin_amdgcn_readfirstlane(v); };
  uni(un); uni(un2); uni(pb); uni(jj); uni(tile64); uni(g); uni(t32); uni(t0); uni(type); uni(slot);
  epoch = __builtin_amdgcn_readfirstlane(epoch);

  v4u wa[21], fl = sent;
  auto wait0 = [&]() INL {
    wait_flags(rs, fl, 0, b, l, epoch, ~0ull, alive);
    if (b == 0 && l == 0) __builtin_amdgcn_raw_buffer_store_b32(epoch, rs, 0, O_EW, 16);
  };
  v4u xs[5];
  f32x4 sx = {0, 0, 0, 0}, sxx = {0, 0, 0, 0};
  auto xs_read = [&]() INL {
#pragma unroll
    for (int i = 0; i < 5; i++) xs[i] = *(LAS v4u*)(lds + L_XH + ((w >> 1) * 9 + (w & 1) * 5 + min(i, 4 - (w & 1))) * 1024 + (tok & 7) * 128 + (((tok >> 3) * 4 + q) ^ (tok & 6)) * 16);
  };
  auto xs_sum = [&]() INL {
#pragma unroll
    for (int i = 0; i < 5; i++) if (i < 4 || !(w & 1)) stat_mm(xs[i], sx, sxx);
  };
  auto xs_put = [&]() INL { stat_put(lds + L_ST + (8 * (w >> 1) + (tok & 7)) * 32 + (w & 1) * 16 + (tok & 8), sx, sxx, l); };
  if (conv) {
    if (w < 4) {
      if (AKP) after_kv();
      __builtin_amdgcn_s_sleep(SLP1);
      v4u tu[8];
      auto off = [&](int i) INL { return ldws(rs, (tid >> 3) * 18432 + (tid & 7) * 16, O_UT + i * 32 * 18432 + pb * 32); };
#pragma unroll
      for (int i = 0; i < 8; i++) tu[i] = off(i);
      auto put = [&](int i, v4u v) INL { *(LAS v2u*)(lds + L_CU + ((tid >> 1) & 3) * CU_SLOT + 160 + 512 * i + (tid >> 3) * 16 + (tid & 1) * 8) = v2u{v.x, v.y}; };
      if constexpr (PPR & 1) settle_each<8>(tu, off, put, [&](v4u v) INL { return ((v.z ^ epoch) | (v.w ^ ~epoch)) != 0; }, alive);
      else settle_tag<8>(rs, tu, off, put, epoch, alive);
    }
    const bool pre = (PREM >> w) & 1, xfirst = (w >> 1) == g;
    if (pre) { after_kv(); sfor<0, 16>(request); }
    if (!(BIN & 1)) bar();
    if (CPRI && w >= 4) __builtin_amdgcn_s_setprio(1);
    LAS char* const ui = lds + L_CU + slot * CU_SLOT;
    LAS char* const zi = lds + L_CX + w * CX_SLOT;
    unsigned dhi, dlo;
    bool again = true;
    auto cside = [&](auto n_, auto r_) INL {
      constexpr int n = decltype(n_)::v, r = decltype(r_)::v;
      if constexpr (n >= CS0 && (n - CS0) % CSP == 0 && (n - CS0) / CSP < 16) {
        constexpr int k = (n - CS0) / CSP;
        if (XO && !xfirst) request(IC<(XO == 1 ? (k < 8 ? 8 + k : k < 11 ? k - 3 : k - 11) : (k < 4 ? 8 + k : k < 7 ? k + 1 : k < 11 ? k + 5 : k - 11))>{}); else if (!pre) request(IC<k>{});
      }
      if constexpr (DEF0 && n == DEF0) {
        constexpr int k = (n - 1) / CSP + 1, y = (CPOS ? 0 : k < 2 ? k : 2) + (k < 3 ? 0 : k < 7 ? k - 3 : 4);
        if (w >= 6) { if (pre) vmwait<CPOS ? 13 : 15>(); else vmwait<9 + y>(); arrive(256); }
      }
      if constexpr (DEF0 && r == 0) {
#pragma nounroll
        while (__builtin_amdgcn_readfirstlane(*(volatile LAS unsigned*)(lds + L_MISC)) != 0x305 && alive > 0) --alive;
        asm volatile("" ::: "memory");
      }
      if constexpr (r == 6) dhi = *(LAS unsigned*)(lds + L_MISC + 16 + slot * 4);
      if constexpr (r == 0) dlo = dhi >> 16;
      if constexpr (CPRI && n == CPRI) __builtin_amdgcn_s_setprio(0);
      if constexpr (CF0 && n == CF0) { if (w == FW) load_flags_async(fl, rs, 0, b, l); }
      if constexpr (r == CFL) {
        constexpr int k0 = (CF0 - CS0) / CSP + 1;
        constexpr int younger = (k0 < 4 ? 4 - k0 : 0) + (k0 <= 5) + (k0 <= 6) + (k0 < 8 ? 4 : k0 < 12 ? 12 - k0 : 0);
        if (w == FW) {
          if (CF0) { flags_ready<younger>(fl); again = flags_bad(fl, epoch) != 0; }
          if (again) load_flags_async(fl, rs, 0, b, l);
        }
      }
      if constexpr (!FAE && r == 0) { if (w == FW) { if (again) flags_ready<0>(fl); wait0(); } }
      if constexpr (BLK && r == CFL + 1) { if (w < 4) vmwait<10>(); else if (un2 >= 0) vmwait<6>(); else vmwait<2>(); }
      if constexpr (!BLK && r == CFL + 1) { if (XO && !xfirst) vmwait<0>(); else if (EML >> w & 1) vmwait<2>(); else if (XW6 && un2 >= 0) vmwait<10>(); else vmwait<CPOS ? 4 : 6>(); }
      if constexpr (XST && r == XSR) xs_read();
      if constexpr (XST == 1 && r == 1) xs_sum();
    };
    auto emit = [&](auto o_, auto e_, auto k_) INL {
      constexpr int o = decltype(o_)::v, e = decltype(e_)::v, k = decltype(k_)::v;
      const f32x4 a = *(LAS f32x4*)(zi + k * 2048 + l * 32), c = *(LAS f32x4*)(zi + k * 2048 + l * 32 + 16);
      const v4u u = *(LAS v4u*)(ui + 160 + o * 128 + k * 1024 + l * 16);
      stws(rs, v4u{pk(dot2(u.x, dlo, a.x), dot2(u.x, dhi, a.y)), pk(dot2(u.y, dlo, a.z), dot2(u.y, dhi, a.w)), pk(dot2(u.z, dlo, c.x), dot2(u.z, dhi, c.y)), pk(dot2(u.w, dlo, c.z), dot2(u.w, dhi, c.w))},
           ((l >> 2) * D * 64 + (l & 3) * 16) | (o + 8 * k + (l >> 3) <= e ? 0 : OOB), O_YV + ((2 * o + 16 * k) * D + pb + slot) * 64);
    };
    LAS char* const ap = lds + L_CF + slot * CF_SLOT + 32 * (l & 3) + 4 * (l & 48) + 2 * (l & 12);
    LAS char* const bp = ui + 224 + 8 * (l & 3) - 4 * (l & 48) - 2 * (l & 12);
    if (type == 0) conv_unit<0, 18>(ap, bp, zi + l * 4, l, cside, emit);
    else if (type == 1) conv_unit<18, 25>(ap, bp, zi + l * 4, l, cside, emit);
    else conv_unit<25, 31>(ap, bp, zi + l * 4, l, cside, emit);
    if (XST == 2) xs_sum();
    if (XST) xs_put();
    if (FAE && w == FW) { if (type == 0) flags_ready<3>(fl); else flags_ready<1>(fl); wait0(); }
    if (CPOS == 1) sfor<0, 3>(clear_own);
  } else {
    constexpr int NTCH = (TCH & 1 ? 2 : 0) + (TCH & 2 ? 3 : 0);
    auto touches = [&]() INL {
      const int xcd = b & 7, q40 = 8 * ((b >> 3) - 27) + w;
      if (TCH & 1) {
#pragma unroll
        for (int i = 0; i < 2; i++) {
          const int n = min(q40 * 128 + i * 64 + l, 4607);
          touch(x + (2 * (16 * (xcd >> 2) + n / 288) + ((xcd >> 1) & 1)) * 32 * D + n % 288 * 64, ldsb + L_CX);
        }
      }
      if (TCH & 2) {
#pragma unroll
        for (int i = 0; i < 3; i++) {
          const int k = 2 * (q40 + 40 * i) + (l >> 5), n = k < 216 ? 8 * (xcd + (k & ~7)) + (k & 7) : 2368 + xcd + 8 * (k - 216);
          touch(main_src(w3, w4, n) + (l & 31) * (n < 1296 ? F : D), ldsb + L_CX);
        }
      }
    };
    if (TCP == 0) { after_kv(); touches(); }
    if (NCG != 2) load2(0, 4);
    if (NCG) {
#pragma nounroll
      while (__builtin_amdgcn_readfirstlane(*(volatile LAS unsigned*)(lds + L_MISC + 12)) == 0 && alive > 0) { asm volatile("s_sleep 1" ::: "memory"); --alive; }
    }
    if (NCG == 2) load2(0, 4);
    main_load(0, 4);
    after_kv();
    sfor<0, 5>([&](auto i_) INL { stage_x5(i_); });
    if (TCP == 1) touches();
    vmwait<8 + (TCP == 1 ? NTCH : 0)>();
    retile(lds + st2(), rs, un2, mdst, 4, l);
    vmwait<4>();
    if (XST) xs_read();
    sfor<0, 3>(clear_own);
    if (XST) { xs_sum(); xs_put(); }
    if (w == 5) { fl = load_flags(rs, 0, b, l); wait0(); }
  }
  bar();

  f32x4 a0 = {0, 0, 0, 0}, a1 = {0, 0, 0, 0}, a2 = {0, 0, 0, 0};
  v4u x1p, pe[NP];
  const bool own_early = ((g & 1 ? 0x67 : 0x33) >> w) & 1;
  const int te = own_early ? w : w == 2 ? 8 : 9, tl = !own_early ? w : w == 2 ? 8 : 9;
  int eo;
  {
    const int pcp = 18 * g + 2 * w + (q >> 1), pcp9 = 18 * g + 16 + (q >> 1), prow = 16 * (q & 1) + tok;
    v4u ty[5], tx[5];
    auto off = [&](int i) INL { return ldws(rs, min(tid + 512 * i, 2303) * 16, O_YV + t32 * 36864); };
    v4u wb[21];
    auto ldw2 = [&](int n) INL { wb[n] = load_gw(rs, O_W2, g, w, l, n); };
    f32x4 cs = {0, 0, 0, 0}, c9 = {0, 0, 0, 0};
    auto ldw1 = [&](int n) INL { const int i = (n + KS) % 21; wa[i] = load_gw(rs, O_W1, g, w, l, i); };
    const v4u ones = {0x3f803f80u, 0x3f803f80u, 0x3f803f80u, 0x3f803f80u};
    {
#pragma unroll
      for (int it = 0; it < 5; it++) tx[it] = *(LAS v4u*)(lds + L_XH + ((2 * rt + (tok >> 3)) * 9 + (min(kq + 4 * it, kq + 12 + (kq < 2 ? 4 : 0)) >> 1)) * 1024 + (tok & 7) * 128 + ((((kq & 1) * 4 + q) ^ (tok & 6)) << 4));
#pragma unroll
      for (int n = 0; n < NWA / 2; n++) ldw1(n);
      SB();
#pragma unroll
      for (int it = 0; it < 5; it++) if (!XST && (it < 4 || kq < 2)) stat_mm(tx[it], sx, sxx);
#pragma unroll
      for (int n = NWA / 2; n < NWA; n++) ldw1(n);
      if (!XST) stat_put(lds + L_ST + (rt * 16 + tok) * 32 + kq * 8, sx, sxx, l);
    }
    if (!XST) bar();
#pragma unroll
    for (int n = 0; n < NA; n++) ldw2(n);
    __builtin_amdgcn_s_sleep(SLP2);
#pragma unroll
    for (int i = 0; i < 5; i++) ty[i] = off(i);
    SB();
    float xf[5][8]; unsigned qm[5][8];
    {
      f32x4 p0 = *(LAS f32x4*)(lds + L_ST + (rt * 16 + tok) * 32), p1 = *(LAS f32x4*)(lds + L_ST + (rt * 16 + tok) * 32 + 16);
      const float mean = ((p0.x + p0.z) + (p1.x + p1.z)) * (1.0f / D);
      const float r = __builtin_amdgcn_rsqf(fmaxf(((p0.y + p0.w) + (p1.y + p1.w)) * (1.0f / D) - mean * mean, 0.f) + EPS), mr = -mean * r;
#pragma unroll
      for (int it = 0; it < 5; it++) {
        const unsigned c[4] = {tx[it].x, tx[it].y, tx[it].z, tx[it].w};
#pragma unroll
        for (int e = 0; e < 4; e++) {
          xf[it][2 * e] = lo(c[e]); xf[it][2 * e + 1] = hi(c[e]);
          const unsigned qq = pk(__builtin_fmaf(xf[it][2 * e], r, mr), __builtin_fmaf(xf[it][2 * e + 1], r, mr));
          qm[it][2 * e] = qq & 0xffffu; qm[it][2 * e + 1] = qq & 0xffff0000u;
          pin(xf[it][2 * e]); pin(xf[it][2 * e + 1]); pin(qm[it][2 * e]); pin(qm[it][2 * e + 1]);
          const int i = 4 * it + e;
#pragma unroll
          for (int n = NWA + (NWB - NWA) * i / 20; n < NWA + (NWB - NWA) * (i + 1) / 20; n++) ldw1(n);
          SB();
        }
      }
      c9 = mfma16(wa[KS], ones, c9); c9 = mfma16(wa[KS + 1], ones, c9);
      if (w < 2) c9 = mfma16(wa[KS + 2], ones, c9);
      *(LAS f32x4*)(lds + L_C9 + w * 64 + q * 16) = c9;
    }
    asm volatile("" ::: "memory");
    if (RTP == 0) retile(lds + L_MS + w * 4096, rs, un, mdst, 0, l);
#pragma unroll
    for (int i = 0; i < 3; i++) *(LAS v4u*)(lds + L_A9B + w * 4096 + i * 1024 + l * 16) = wa[KS + i];
    if (R2P == 0 && conv && un2 >= 0) retile(lds + st2(), rs, un2, mdst, 4, l);
    settle_s<1, 5>(rs, ty, off, [&](int i, v4u v) INL { const int n = min(tid + 512 * i, 2303); *(LAS v4u*)(lds + L_Y + n * 16 + (n / 576) * 32) = v; }, alive);
    vmwait<NWB - NWA>();
    bar();
    if (w == F3) publish_flag(rs, 3, 4 * t32 + g, l, epoch);
    const int a = (l & 15) >> 2, bb = l & 3;
    f32x4 sh = {0, 0, 0, 0}, shh = {0, 0, 0, 0};
    v2u y0[5], y1[5];
#pragma unroll
    for (int it = 0; it < 5; it++) {
      LAS char* p = lds + L_Y + (8 * min(kq + 4 * it, KS - 1) + 2 * q) * 64 + a * (144 * 64 + 32) + rt * 32 + bb * 8;
      y0[it] = trrd(p); y1[it] = trrd(p + 64);
    }
    if (W1F) {
#pragma unroll
      for (int n = NWB; n < 21; n++) ldw1(n);
    }
    if (RTP == 1) retile(lds + L_MS + w * 4096, rs, un, mdst, 0, l);
    if (R2P == 1 && conv && un2 >= 0) retile(lds + st2(), rs, un2, mdst, 4, l);
#pragma unroll
    for (int n = NA; n < NA + NB; n++) ldw2(n);
#pragma unroll
    for (int it = 0; it < 5; it++) if (it < 4 || kq < 2) {
      const int ks = kq + 4 * it;
      const unsigned yy[4] = {y0[it].x, y0[it].y, y1[it].x, y1[it].y};
      unsigned hp[4];
#pragma unroll
      for (int e = 0; e < 4; e++) hp[e] = pk(dot2(qm[it][2 * e], yy[e], xf[it][2 * e]), dot2(qm[it][2 * e + 1], yy[e], xf[it][2 * e + 1]));
      const v4u hv = {hp[0], hp[1], hp[2], hp[3]};
      *(LAS v4u*)(lds + L_XH + (rt * KS + ks) * 1024 + l * 16) = hv;
      stat_mm(hv, sh, shh);
      if (it < 4) {
#pragma unroll
        for (int i = (NWB - 3) * it / 4; i < (NWB - 3) * (it + 1) / 4; i++) cs = mfma16(wa[i], ones, cs);
        if (!W1F) {
#pragma unroll
          for (int n = NWB + (21 - NWB) * it / 4; n < NWB + (21 - NWB) * (it + 1) / 4; n++) ldw1(n);
          SB();
        }
      }
    }
    stat_put(lds + L_ST + 1024 + (rt * 16 + tok) * 32 + kq * 8, sh, shh, l);
    if (RTP == 0) vmwait<NB>();
    auto sum9 = [&]() INL {
      c9 = *(LAS f32x4*)(lds + L_C9 + q * 16);
#pragma unroll
      for (int i = 1; i < 8; i++) c9 += *(LAS f32x4*)(lds + L_C9 + i * 64 + q * 16);
    };
    if (!(BIN & 8)) bar();
    unsigned cnt1 = 0;
    f32x4 gc[2]; float r1[2], mr1[2];
    auto g1pre = [&](bool nine, bool other) INL {
      if (RTP == 2) retile(lds + L_MS + w * 4096, rs, un, mdst, 0, l);
      if (R2P == 2 && other && conv && un2 >= 0) retile(lds + st2(), rs, un2, mdst, 4, l);
      if (RTP == 0 && w == 7) publish_flag(rs, 1, b, l, epoch);
      if (nine && !(BIN & 8)) sum9();
#pragma unroll
      for (int m = 0; m < 2; m++) {
        f32x4 p0 = *(LAS f32x4*)(lds + L_ST + 1024 + (m * 16 + tok) * 32), p1 = *(LAS f32x4*)(lds + L_ST + 1024 + (m * 16 + tok) * 32 + 16);
        gc[m] = p0 + p1;
      }
    };
    static_assert(U2W >= 2, "second units");
    if (!(BIN & 8)) g1pre(w < 2, true);
    small_gemm<0>(wa, lds + L_XH, lds + L_A9B + l * 16, w, l, a0, a1, a2, [&](auto k_) INL {
      constexpr int ks = decltype(k_)::v;
      if constexpr (ks < 0) g1pre(ks == -2, ks == -1);
      if constexpr ((BIN & 8) && ks == 16) { if (w < 2) sum9(); }
      if constexpr (RTP && ks == RTK) {
        vmwait<(RTP == 1 && R2P != 2 ? NB : 0) + NC * (ks < 14 ? ks : 14) / 14>();
        if (l == 0) cnt1 = __hip_atomic_fetch_add((LAS unsigned*)(lds + L_MISC + 4), 1u, __ATOMIC_RELAXED, __HIP_MEMORY_SCOPE_WORKGROUP);
      }
      if constexpr (ks >= NWB - 3) cs = mfma16(wa[ks], ones, cs);
      if constexpr (ks == F3K) { if (w == F3) fl = load_flags(rs, 3, b, l); }
      if constexpr (RTP && ks == RTK + 2) { if (__builtin_amdgcn_readfirstlane(cnt1) == 7) publish_flag(rs, 1, b, l, epoch); }
      if constexpr (ks >= 0 && ks < 14) { sfor<NC * ks / 14, NC * (ks + 1) / 14>([&](auto n_) INL { ldw2(NA + NB + decltype(n_)::v); }); }
      if constexpr (ks == 4 || ks == 8) {
        constexpr int m = ks == 8;
        const float mean = (gc[m].x + gc[m].z) * (1.0f / D);
        const float var = fmaxf((gc[m].y + gc[m].w) * (1.0f / D) - mean * mean, 0.f);
        r1[m] = __builtin_amdgcn_rsqf(var + EPS); mr1[m] = -mean * r1[m];
        const float v2 = var * r1[m] * r1[m] + EPS, r2 = __builtin_amdgcn_rsqf(v2);
        gc[m] = gelu_consts(r1[m] * r2, (var + EPS) * r1[m] * v2 * r2);
        gc[m].w = -mean;
      }
    });
    const int dl = g == 0 ? 1 : g == 1 ? -1 : g == 2 ? 2 : -2;
    int tt = t0 + prow + dl;
    const bool wrap = tt < 0 || tt >= S;
    tt &= S - 1;
    auto gl = [&](f32x4 v, f32x4 c, f32x4 k) INL { v += c * k.w; return f32x4{gelu_ln(v.x, k), gelu_ln(v.y, k), gelu_ln(v.z, k), gelu_ln(v.w, k)}; };
    {
      v4u o = piece2(gl(a0, cs, gc[0]), gl(a1, cs, gc[1]));
      if (wrap) o = v4u{0, 0, 0, 0};
      stws(rs, o, frag_off(tt, pcp), O_HSH);
    }
    if (w < 2) {
      f32x4 t = gl(a2, c9, w ? gc[1] : gc[0]);
      v4u o = piece2(t, t);
      if (wrap) o = v4u{0, 0, 0, 0};
      if ((q & 1) == w) stws(rs, o, frag_off(tt, pcp9), O_HSH);
    }
    if (w == F3) wait_flags(rs, fl, 3, b, l, epoch, (1ull << t32) | (1ull << ((t32 + 1) & 63)) | (1ull << ((t32 - 1) & 63)), alive);
    bar();
#pragma unroll
    for (int n = NA + NB + NC; n < 21; n++) ldw2(n);
    const v4u mm0 = ldws(rs, min(tid >> 1, 71) * 32, O_M2), mm1 = ldws(rs, min(tid >> 1, 71) * 32 + 16, O_M2);
    SB();
    auto xbinit = [&](int m, int col) INL {
      v2u hh = *(LAS v2u*)(lds + L_XH + frag_off(16 * m + tok, col >> 3) + (q & 1) * 8);
      const float r = m ? r1[1] : r1[0], mr = m ? mr1[1] : mr1[0];
      return f32x4{__builtin_fmaf(lo(hh.x), r, mr), __builtin_fmaf(hi(hh.x), r, mr), __builtin_fmaf(lo(hh.y), r, mr), __builtin_fmaf(hi(hh.y), r, mr)};
    };
    a0 = xbinit(0, 144 * g + 16 * w + 4 * q); a1 = xbinit(1, 144 * g + 16 * w + 4 * q);
    a2 = w < 2 ? xbinit(w, 144 * g + 128 + 4 * q) : f32x4{0, 0, 0, 0};
    *(LAS v4u*)(lds + L_HS + w * 1024 + l * 16) = *(LAS v4u*)(lds + L_XH + frag_off(prow, pcp));
    if (w == 2) *(LAS v4u*)(lds + L_HS + 8192 + l * 16) = *(LAS v4u*)(lds + L_XH + frag_off(prow, pcp9));
    pin(a0); pin(a1); pin(a2);
    v4u tb[5];
    auto off2 = [&](int i) INL { return ldws(rs, min(tid + 512 * i, 2303) * 16, O_HSH + t32 * 36864); };
    SB();
    __builtin_amdgcn_s_sleep(SLP3);
#pragma unroll
    for (int i = 0; i < 5; i++) tb[i] = off2(i);
    if (w == 7) fl = load_flags(rs, 1, b, l);
#pragma unroll
    for (int i = 0; i < 3; i++) *(LAS v4u*)(lds + L_A9B + ks9(w, i) * 1152 + l * 16) = wb[KS + i];
    if (tid < 144) {
      const unsigned sel = tid & 1 ? 0x07060302u : 0x05040100u;
      *(LAS v4u*)(lds + L_A9B + (tid >> 3) * 1152 + 1024 + (tid & 7) * 16) =
          v4u{__builtin_amdgcn_perm(mm0.y, mm0.x, sel), __builtin_amdgcn_perm(mm0.w, mm0.z, sel), __builtin_amdgcn_perm(mm1.y, mm1.x, sel), __builtin_amdgcn_perm(mm1.w, mm1.z, sel)};
    }
    settle_s<2, 5>(rs, tb, off2, [&](int i, v4u v) INL { *(LAS v4u*)(lds + L_B2 + min(tid + 512 * i, 2303) * 16) = v; }, alive);
    if (w == 7) wait_flags(rs, fl, 1, b, l, epoch, ~0ull, alive);
    if (!(BIN & 2)) bar();
    v4u ring[RW];
    const int sbase = O_WM + (jj * 1296 + (w < 4 ? w * 180 : 144 + w * 144)) * 1024;
    unsigned wua = 0, wub = 0;
    const int wf1 = w < 4 ? WF1 : WF1 * 4 / 5, wf2 = w < 4 ? 180 : 144;
    auto warm_at = [&](int p) INL {
      if (WPA == p) wua = warm(rs, sbase, l, tile64 & 15, WF0, wf1);
      if (WPB == p) wub = warm(rs, sbase, l, tile64 & 15, wf1, wf2);
    };
    warm_at(1);
    small_gemm<1>(wb, lds + L_B2, lds + L_A9B + (w < 2 ? l * 16 : 1024 + (2 * q + (tok & 1)) * 16), w, l, a0, a1, a2, [&](auto k_) INL {
      constexpr int ks = decltype(k_)::v;
      if constexpr (ks >= 0 && ks < 14) { sfor<ND * ks / 14, ND * (ks + 1) / 14>([&](auto n_) INL { ring[decltype(n_)::v] = ring_load(rs, sbase, l * 16, decltype(n_)::v); }); }
    });
    if ((w & 6) == 2) *(LAS float*)(lds + L_C9 + (w & 1) * 64 + tok * 4) = a2.x + a2.y;
    x1p = piece2(a0, a1);
    if (w < 2) { const v4u o = piece2(a2, a2); if ((q & 1) == w) *(LAS v4u*)(lds + L_HS + 9216 + l * 16) = o; }
    bar();
    {
      const float mu0 = *(LAS float*)(lds + L_C9 + tok * 4), mu1 = *(LAS float*)(lds + L_C9 + 64 + tok * 4);
      stws(rs, piece2(a0 - mu0, a1 - mu1), frag_off(t0 + prow, pcp), O_X1);
      if (w < 2) { a2 -= w ? mu1 : mu0; const v4u o = piece2(a2, a2); if ((q & 1) == w) stws(rs, o, frag_off(t0 + prow, pcp9), O_X1); }
    }
    bar();
#pragma unroll
    for (int i = ND; i < RP; i++) ring[i] = ring_load(rs, sbase, l * 16, i);
    warm_at(2);
    __builtin_amdgcn_s_sleep(SLP4);
    v4u tq[9];
    const int xo = ((l >> 3) & 3) * 256 + (8 * (w & 1) + (l & 7)) * 16;
    auto off3 = [&](int i) INL { return ldws(rs, (l >> 5) * 1024 + xo, O_X1 + ((4 * tile64 + (w >> 1)) * KS + 2 * i) * 1024); };
#pragma unroll
    for (int i = 0; i < 9; i++) tq[i] = off3(i);
#pragma unroll
    for (int i = RP; i < RQ; i++) ring[i] = ring_load(rs, sbase, l * 16, i);
    warm_at(3);
    SB();
    settle_s<3, 9>(rs, tq, off3, [&](int i, v4u v) INL { *(LAS v4u*)(lds + L_X + (8 * i + (w >> 1)) * 1024 + (l >> 5) * 4096 + xo) = v; }, alive);
#pragma unroll
    for (int i = RQ; i < RW; i++) ring[i] = ring_load(rs, sbase, l * 16, i);
    if (WPA == 1 || WPA == 2) asm volatile("" :: "v"(wua));
    if (WPB == 1 || WPB == 2) asm volatile("" :: "v"(wub));
    if (!(BIN & 4)) bar();
    eo = frag_off(t0 + prow, 18 * g + 2 * te + (q >> 1)) | (te == 9 ? OOB : 0);
    if (w >= 4) {
      mlp_body<2>(lds, rs, ring, tq, sbase, b, jj, tile64, w, epoch, fl, pe, eo, t32 & ~1, wua, wub);
#pragma unroll
      for (int i = 0; i < RW; i++) asm volatile("" : "=v"(ring[i]));
#pragma unroll
      for (int i = 0; i < 9; i++) asm volatile("" : "=v"(tq[i]));
      if (WPA == 3) asm volatile("" : "=v"(wua));
      if (WPB == 3) asm volatile("" : "=v"(wub));
    }
    int wv = w;
    asm volatile("" : "+s"(wv));
    if (wv < 4) mlp_body<3>(lds, rs, ring, tq, sbase, b, jj, tile64, w, epoch, fl, pe, eo, t32 & ~1, wua, wub);
  }
  {
    const int l = lane(), tok = l & 15, q = l >> 4, prow = 16 * (q & 1) + tok;
    unsigned sel_lo = 0x3f80u, sel_hi = 0x3f800000u;
    asm volatile("" : "+s"(sel_lo), "+s"(sel_hi));
    if (TW) {
      wait_flags(rs, fl, 2, b, (t32 & ~1) + (l & 1), epoch, ~0ull, alive);
#pragma unroll
      for (int i = 0; i < NP; i++) pe[i] = ldws(rs, eo, part_base(jj, i));
    }
    bar();
    if (!TW && flags_bad(fl, epoch)) {
      wait_flags(rs, fl, 2, b, (t32 & ~1) + (l & 1), epoch, ~0ull, alive);
      if (te != 9) {
#pragma unroll
        for (int i = 0; i < NP; i++) pe[i] = ldws(rs, eo & ~OOB, part_base(jj, i));
      }
    }
    __builtin_amdgcn_s_sleep(SLP5);
    auto finish = [&](v4u (&pp)[NP], int t, auto poll) INL {
      const v4u hh = *(LAS v4u*)(lds + L_HS + t * 1024 + l * 16), x9 = *(LAS v4u*)(lds + L_HS + 9216 + l * 16), xx = t == 8 ? x9 : x1p;
      float s[8] = {lo(hh.x) + lo(xx.x), hi(hh.x) + hi(xx.x), lo(hh.y) + lo(xx.y), hi(hh.y) + hi(xx.y),
                    lo(hh.z) + lo(xx.z), hi(hh.z) + hi(xx.z), lo(hh.w) + lo(xx.w), hi(hh.w) + hi(xx.w)};
      auto add = [&](v4u v) INL {
        s[0] = dot2(v.x, sel_lo, s[0]); s[1] = dot2(v.x, sel_hi, s[1]); s[2] = dot2(v.y, sel_lo, s[2]); s[3] = dot2(v.y, sel_hi, s[3]);
        s[4] = dot2(v.z, sel_lo, s[4]); s[5] = dot2(v.z, sel_hi, s[5]); s[6] = dot2(v.w, sel_lo, s[6]); s[7] = dot2(v.w, sel_hi, s[7]);
      };
      if (OWN == 2) add(*(LAS v4u*)(lds + L_X + frag_off((t0 & 32) + prow, 18 * g + 2 * t + (q >> 1))));
#pragma unroll
      for (int i = 0; i < 8; i++) pin(s[i]);
      poll();
#pragma unroll
      for (int i = 0; i < NP; i++) add(pp[i]);
      const v4u ov = {pk(s[0], s[1]), pk(s[2], s[3]), pk(s[4], s[5]), pk(s[6], s[7])};
      __builtin_amdgcn_raw_buffer_store_b128(ov, __builtin_amdgcn_make_buffer_rsrc((void*)out, 0, ACT, 0x00020000), ((t0 + prow) * D + (18 * g + 2 * t + (q >> 1)) * 8) * 2, 0, OUTST);
      asm volatile("s_nop 1" :: "v"(ov));
    };
    v4u pl[NP];
    const int po = frag_off(t0 + prow, 18 * g + 2 * tl + (q >> 1)) | (tl == 9 ? OOB : 0);
    auto off = [&](int i) INL { return ldws(rs, po, part_base(jj, i)); };
#pragma unroll
    for (int i = 0; i < NP; i++) pl[i] = off(i);
    SB();
    if (te != 9) finish(pe, te, [&]() INL {});
    if (tl != 9) finish(pl, tl, [&]() INL { settle_s<4, NP>(rs, pl, off, [&](int, v4u) INL {}, alive); });
  }
  asm volatile(".Lpf_end:");
}

extern "C" void kernel_launch(void* const* d_in, const int* in_sizes, int n_in, void* d_out, int out_size, void* d_ws, size_t ws_size,
                              hipStream_t stream) {
  mega<<<256, 512, 0, stream>>>((const u16*)d_in[0], (const u16*)d_in[1], (const u16*)d_in[2], (const u16*)d_in[3], (const u16*)d_in[4],
                                (const u16*)d_in[5], (const u16*)d_in[7], (const u16*)d_in[8], (const u16*)d_in[9], (const u16*)d_in[10],
                                (char*)d_ws, (u16*)d_out);
}
```
